```python
import torch

def _softmax(x, axis=-1):
    return torch.softmax(x, dim=axis)
def _gelu(x):
    return torch.nn.functional.gelu(x, approximate="tanh")
def _silu(x):
    return torch.nn.functional.silu(x)
def _sigmoid(x):
    return torch.sigmoid(x)
def _layernorm(x, axis=-1, eps=1e-5):
    m = x.mean(dim=axis, keepdim=True)
    v = x.var(dim=axis, keepdim=True, unbiased=False)
    return (x - m) / torch.sqrt(v + eps)
def _scatter_add(tgt, idx, src, axis=0):
    return tgt.clone().index_add_(axis, idx, src)
def _segment_sum(src, seg, n):
    out = torch.zeros((n,) + src.shape[1:], dtype=src.dtype)
    return out.index_add_(0, seg, src)
def _pad(x, pw):
    flat = tuple(v for pair in reversed(pw) for v in pair)
    return torch.nn.functional.pad(x, flat)

kernel_name = 'interleave_layers__spectral_gfnet__dense_activation_gelu__prompt'

def setup_inputs():

    _w = (lambda a: (lambda t: t.to(torch.bfloat16) if t.is_floating_point() else t)(torch.as_tensor(a)))
    def _A_setup():
        B, S, D = 1, 2048, 1024
        Df = D // 2 + 1
        return {
            "x": _w((lambda *s: torch.randn(*s, dtype=torch.bfloat16) * 0.05)(B, S, D)),
            "w_r": _w((lambda *s: torch.randn(*s, dtype=torch.bfloat16) * 0.4)(S, Df) * 0.02),
            "w_i": _w((lambda *s: torch.randn(*s, dtype=torch.bfloat16) * 0.4)(S, Df) * 0.02),
        }
    _A_in = _A_setup()

    def _B_setup():
        B, S, D, F = 1, 2048, 1024, 3584
        return {
            "x": _w((lambda *s: torch.randn(*s, dtype=torch.bfloat16) * 0.05)(B, S, D)),
            "w1": _w((lambda *s: torch.randn(*s, dtype=torch.bfloat16) * 0.4)(D, F) * (D ** -0.5)),
            "b1": _w((lambda *s: torch.randn(*s, dtype=torch.bfloat16) * 12.8)(F) * 0.01),
            "w2": _w((lambda *s: torch.randn(*s, dtype=torch.bfloat16) * 0.1)(F, D) * (F ** -0.5)),
            "b2": _w((lambda *s: torch.randn(*s, dtype=torch.bfloat16) * 0.8)(D) * 0.01),
        }
    _B_in = _B_setup()

    return {
        "A_x": _A_in["x"],
        "A_w_r": _A_in["w_r"],
        "A_w_i": _A_in["w_i"],
        "B_x": _B_in["x"],
        "B_w1": _B_in["w1"],
        "B_b1": _B_in["b1"],
        "B_w2": _B_in["w2"],
        "B_b2": _B_in["b2"],
    }

def reference(A_x, A_w_r, A_w_i, B_x, B_w1, B_b1, B_w2, B_b2):
    def _A_ref(x, w_r, w_i):
        B, S, D = x.shape
        X = (lambda x, axes=(-2,-1): torch.fft.rfft2(x.float(), dim=axes))(x, axes=(-2, -1))
        W = w_r + 1j * w_i
        X = X * W
        y = (lambda x, s, axes=(-2,-1): torch.fft.irfft2(x, s=s, dim=axes).to(torch.bfloat16))(X, (S, D), axes=(-2, -1))
        return y

    def _B_ref(x, w1, b1, w2, b2):
        h = torch.einsum("bsd,df->bsf", x, w1) + b1
        h = _gelu(h)
        return torch.einsum("bsf,fd->bsd", h, w2) + b2

    x = A_x
    x = x + _A_ref(_layernorm(x, axis=-1), A_w_r, A_w_i)
    x = x + _B_ref(_layernorm(x, axis=-1), B_w1, B_b1, B_w2, B_b2)
    x = x + _A_ref(_layernorm(x, axis=-1), A_w_r, A_w_i)
    x = x + _B_ref(_layernorm(x, axis=-1), B_w1, B_b1, B_w2, B_b2)
    return x
```

```cpp
#include <hip/hip_runtime.h>
#include <hip/hip_bf16.h>

typedef unsigned short u16;
typedef short s16x4 __attribute__((ext_vector_type(4)));
typedef short s16x8 __attribute__((ext_vector_type(8)));
typedef __bf16 b16x8 __attribute__((ext_vector_type(8)));
typedef __bf16 b16x2 __attribute__((ext_vector_type(2)));
typedef _Float16 h16x8 __attribute__((ext_vector_type(8)));
typedef _Float16 h16x4 __attribute__((ext_vector_type(4)));
typedef _Float16 h16x2 __attribute__((ext_vector_type(2)));
typedef float f32x4 __attribute__((ext_vector_type(4)));
typedef float f32x2 __attribute__((ext_vector_type(2)));
typedef unsigned u32x4 __attribute__((ext_vector_type(4)));
typedef unsigned u32x2 __attribute__((ext_vector_type(2)));
#define LAS __attribute__((address_space(3)))
#define GAS __attribute__((address_space(1)))
#define DI __device__ __forceinline__

__device__ const unsigned __attribute__((aligned(1024))) g_tab[26 * 256] = {
0x3c003c00,0x3c003c00,0x3c003c00,0x3c003c00,0x3bd93c00,0x3aa73b64,0x387239a8,0x323e361f,0x3b643c00,0x361f39a8,0xb61f0000,0xbb64b9a8,0x3aa73c00,0xb23e361f,0xbbd9b9a8,0xb872bb64,
0x39a83c00,0xb9a80000,0xb9a8bc00,0x39a80000,0x38723c00,0xbbd9b61f,0x323eb9a8,0x3aa73b64,0x361f3c00,0xbb64b9a8,0x3b640000,0xb61f39a8,0x323e3c00,0xb872bb64,0x3aa739a8,0xbbd9b61f,
0x3c00,0xbc00,0x3c00,0xbc00,0xb23e3c00,0x3872bb64,0xbaa739a8,0x3bd9b61f,0xb61f3c00,0x3b64b9a8,0xbb640000,0x361f39a8,0xb8723c00,0x3bd9b61f,0xb23eb9a8,0xbaa73b64,
0xb9a83c00,0x39a80000,0x39a8bc00,0xb9a80000,0xbaa73c00,0x323e361f,0x3bd9b9a8,0x3872bb64,0xbb643c00,0xb61f39a8,0x361f0000,0x3b64b9a8,0xbbd93c00,0xbaa73b64,0xb87239a8,0xb23e361f,
0x3c003c00,0x3c003c00,0x3c003c00,0x3c003c00,0xb23e0000,0xb872b61f,0xbaa7b9a8,0xbbd9bb64,0xbb64bc00,0xb61fb9a8,0x361f0000,0x3b6439a8,0x38720000,0x3bd93b64,0x323e39a8,0xbaa7b61f,
0x39a83c00,0xb9a80000,0xb9a8bc00,0x39a80000,0xbaa70000,0xb23ebb64,0x3bd939a8,0xb872361f,0xb61fbc00,0x3b6439a8,0xbb640000,0x361fb9a8,0x3bd90000,0xbaa7361f,0x3872b9a8,0xb23e3b64,
0x3c00,0xbc00,0x3c00,0xbc00,0xbbd90000,0x3aa7361f,0xb872b9a8,0x323e3b64,0x361fbc00,0xbb6439a8,0x3b640000,0xb61fb9a8,0x3aa70000,0x323ebb64,0xbbd939a8,0x3872361f,
0xb9a83c00,0x39a80000,0x39a8bc00,0xb9a80000,0xb8720000,0xbbd93b64,0xb23e39a8,0x3aa7b61f,0x3b64bc00,0x361fb9a8,0xb61f0000,0xbb6439a8,0x323e0000,0x3872b61f,0x3aa7b9a8,0x3bd9bb64,
0x3c003c00,0x3c003c00,0x3c003c00,0x3c003c00,0xbbd9bc00,0xbaa7bb64,0xb872b9a8,0xb23eb61f,0x3b643c00,0x361f39a8,0xb61f0000,0xbb64b9a8,0xbaa7bc00,0x323eb61f,0x3bd939a8,0x38723b64,
0x39a83c00,0xb9a80000,0xb9a8bc00,0x39a80000,0xb872bc00,0x3bd9361f,0xb23e39a8,0xbaa7bb64,0x361f3c00,0xbb64b9a8,0x3b640000,0xb61f39a8,0xb23ebc00,0x38723b64,0xbaa7b9a8,0x3bd9361f,
0x3c00,0xbc00,0x3c00,0xbc00,0x323ebc00,0xb8723b64,0x3aa7b9a8,0xbbd9361f,0xb61f3c00,0x3b64b9a8,0xbb640000,0x361f39a8,0x3872bc00,0xbbd9361f,0x323e39a8,0x3aa7bb64,
0xb9a83c00,0x39a80000,0x39a8bc00,0xb9a80000,0x3aa7bc00,0xb23eb61f,0xbbd939a8,0xb8723b64,0xbb643c00,0xb61f39a8,0x361f0000,0x3b64b9a8,0x3bd9bc00,0x3aa7bb64,0x3872b9a8,0x323eb61f,
0x3c003c00,0x3c003c00,0x3c003c00,0x3c003c00,0x323e0000,0x3872361f,0x3aa739a8,0x3bd93b64,0xbb64bc00,0xb61fb9a8,0x361f0000,0x3b6439a8,0xb8720000,0xbbd9bb64,0xb23eb9a8,0x3aa7361f,
0x39a83c00,0xb9a80000,0xb9a8bc00,0x39a80000,0x3aa70000,0x323e3b64,0xbbd9b9a8,0x3872b61f,0xb61fbc00,0x3b6439a8,0xbb640000,0x361fb9a8,0xbbd90000,0x3aa7b61f,0xb87239a8,0x323ebb64,
0x3c00,0xbc00,0x3c00,0xbc00,0x3bd90000,0xbaa7b61f,0x387239a8,0xb23ebb64,0x361fbc00,0xbb6439a8,0x3b640000,0xb61fb9a8,0xbaa70000,0xb23e3b64,0x3bd9b9a8,0xb872b61f,
0xb9a83c00,0x39a80000,0x39a8bc00,0xb9a80000,0x38720000,0x3bd9bb64,0x323eb9a8,0xbaa7361f,0x3b64bc00,0x361fb9a8,0xb61f0000,0xbb6439a8,0xb23e0000,0xb872361f,0xbaa739a8,0xbbd93b64,
0xbc003c00,0xbc003c00,0xbc003c00,0xbc003c00,0xb23e8000,0xb872b61f,0xbaa7b9a8,0xbbd9bb64,0xb61f8000,0xbb64b9a8,0xbb64bc00,0xb61fb9a8,0xb8728000,0xbbd9bb64,0xb23eb9a8,0x3aa7361f,
0xb9a88000,0xb9a8bc00,0x39a88000,0x39a83c00,0xbaa78000,0xb23ebb64,0x3bd939a8,0xb872361f,0xbb648000,0x361fb9a8,0x361f3c00,0xbb64b9a8,0xbbd98000,0x3aa7b61f,0xb87239a8,0x323ebb64,
0xbc008000,0x3c008000,0xbc008000,0x3c008000,0xbbd98000,0x3aa7361f,0xb872b9a8,0x323e3b64,0xbb648000,0x361f39a8,0x361fbc00,0xbb6439a8,0xbaa78000,0xb23e3b64,0x3bd9b9a8,0xb872b61f,
0xb9a88000,0xb9a83c00,0x39a88000,0x39a8bc00,0xb8728000,0xbbd93b64,0xb23e39a8,0x3aa7b61f,0xb61f8000,0xbb6439a8,0xbb643c00,0xb61f39a8,0xb23e8000,0xb872361f,0xbaa739a8,0xbbd93b64,
0xbc003c00,0xbc003c00,0xbc003c00,0xbc003c00,0xbbd9bc00,0xbaa7bb64,0xb872b9a8,0xb23eb61f,0x361f8000,0x3b6439a8,0x3b643c00,0x361f39a8,0x3aa73c00,0xb23e361f,0xbbd9b9a8,0xb872bb64,
0xb9a88000,0xb9a8bc00,0x39a88000,0x39a83c00,0xb872bc00,0x3bd9361f,0xb23e39a8,0xbaa7bb64,0x3b648000,0xb61f39a8,0xb61fbc00,0x3b6439a8,0x323e3c00,0xb872bb64,0x3aa739a8,0xbbd9b61f,
0xbc008000,0x3c008000,0xbc008000,0x3c008000,0x323ebc00,0xb8723b64,0x3aa7b9a8,0xbbd9361f,0x3b648000,0xb61fb9a8,0xb61f3c00,0x3b64b9a8,0xb8723c00,0x3bd9b61f,0xb23eb9a8,0xbaa73b64,
0xb9a88000,0xb9a83c00,0x39a88000,0x39a8bc00,0x3aa7bc00,0xb23eb61f,0xbbd939a8,0xb8723b64,0x361f8000,0x3b64b9a8,0x3b64bc00,0x361fb9a8,0xbbd93c00,0xbaa73b64,0xb87239a8,0xb23e361f,
0xbc003c00,0xbc003c00,0xbc003c00,0xbc003c00,0x323e8000,0x3872361f,0x3aa739a8,0x3bd93b64,0xb61f8000,0xbb64b9a8,0xbb64bc00,0xb61fb9a8,0x38728000,0x3bd93b64,0x323e39a8,0xbaa7b61f,
0xb9a88000,0xb9a8bc00,0x39a88000,0x39a83c00,0x3aa78000,0x323e3b64,0xbbd9b9a8,0x3872b61f,0xbb648000,0x361fb9a8,0x361f3c00,0xbb64b9a8,0x3bd98000,0xbaa7361f,0x3872b9a8,0xb23e3b64,
0xbc008000,0x3c008000,0xbc008000,0x3c008000,0x3bd98000,0xbaa7b61f,0x387239a8,0xb23ebb64,0xbb648000,0x361f39a8,0x361fbc00,0xbb6439a8,0x3aa78000,0x323ebb64,0xbbd939a8,0x3872361f,
0xb9a88000,0xb9a83c00,0x39a88000,0x39a8bc00,0x38728000,0x3bd9bb64,0x323eb9a8,0xbaa7361f,0xb61f8000,0xbb6439a8,0xbb643c00,0xb61f39a8,0x323e8000,0x3872b61f,0x3aa7b9a8,0x3bd9bb64,
0xbc003c00,0xbc003c00,0xbc003c00,0xbc003c00,0x3bd93c00,0x3aa73b64,0x387239a8,0x323e361f,0x361f8000,0x3b6439a8,0x3b643c00,0x361f39a8,0xbaa7bc00,0x323eb61f,0x3bd939a8,0x38723b64,
0xb9a88000,0xb9a8bc00,0x39a88000,0x39a83c00,0x38723c00,0xbbd9b61f,0x323eb9a8,0x3aa73b64,0x3b648000,0xb61f39a8,0xb61fbc00,0x3b6439a8,0xb23ebc00,0x38723b64,0xbaa7b9a8,0x3bd9361f,
0xbc008000,0x3c008000,0xbc008000,0x3c008000,0xb23e3c00,0x3872bb64,0xbaa739a8,0x3bd9b61f,0x3b648000,0xb61fb9a8,0xb61f3c00,0x3b64b9a8,0x3872bc00,0xbbd9361f,0x323e39a8,0x3aa7bb64,
0xb9a88000,0xb9a83c00,0x39a88000,0x39a8bc00,0xbaa73c00,0x323e361f,0x3bd9b9a8,0x3872bb64,0x361f8000,0x3b64b9a8,0x3b64bc00,0x361fb9a8,0x3bd9bc00,0x3aa7bb64,0x3872b9a8,0x323eb61f,
0x3c003c00,0x3c003c00,0x3c003c00,0x3c003c00,0x3bd93c00,0x3aa73b64,0xbbd9bc00,0xbaa7bb64,0x3b643c00,0x361f39a8,0x3b643c00,0x361f39a8,0x3aa73c00,0xb23e361f,0xbaa7bc00,0x323eb61f,
0x39a83c00,0xb9a80000,0x39a83c00,0xb9a80000,0x38723c00,0xbbd9b61f,0xb872bc00,0x3bd9361f,0x361f3c00,0xbb64b9a8,0x361f3c00,0xbb64b9a8,0x323e3c00,0xb872bb64,0xb23ebc00,0x38723b64,
0x3c00,0xbc00,0x3c00,0xbc00,0xb23e3c00,0x3872bb64,0x323ebc00,0xb8723b64,0xb61f3c00,0x3b64b9a8,0xb61f3c00,0x3b64b9a8,0xb8723c00,0x3bd9b61f,0x3872bc00,0xbbd9361f,
0xb9a83c00,0x39a80000,0xb9a83c00,0x39a80000,0xbaa73c00,0x323e361f,0x3aa7bc00,0xb23eb61f,0xbb643c00,0xb61f39a8,0xbb643c00,0xb61f39a8,0xbbd93c00,0xbaa73b64,0x3bd9bc00,0x3aa7bb64,
0x3c003c00,0x3c003c00,0x3c003c00,0x3c003c00,0x387239a8,0x323e361f,0xb872b9a8,0xb23eb61f,0xb61f0000,0xbb64b9a8,0xb61f0000,0xbb64b9a8,0xbbd9b9a8,0xb872bb64,0x3bd939a8,0x38723b64,
0xb9a8bc00,0x39a80000,0xb9a8bc00,0x39a80000,0x323eb9a8,0x3aa73b64,0xb23e39a8,0xbaa7bb64,0x3b640000,0xb61f39a8,0x3b640000,0xb61f39a8,0x3aa739a8,0xbbd9b61f,0xbaa7b9a8,0x3bd9361f,
0x3c00,0xbc00,0x3c00,0xbc00,0xbaa739a8,0x3bd9b61f,0x3aa7b9a8,0xbbd9361f,0xbb640000,0x361f39a8,0xbb640000,0x361f39a8,0xb23eb9a8,0xbaa73b64,0x323e39a8,0x3aa7bb64,
0x39a8bc00,0xb9a80000,0x39a8bc00,0xb9a80000,0x3bd9b9a8,0x3872bb64,0xbbd939a8,0xb8723b64,0x361f0000,0x3b64b9a8,0x361f0000,0x3b64b9a8,0xb87239a8,0xb23e361f,0x3872b9a8,0x323eb61f,
0x3c003c00,0x3c003c00,0x3c003c00,0x3c003c00,0xb23e0000,0xb872b61f,0x323e0000,0x3872361f,0xbb64bc00,0xb61fb9a8,0xbb64bc00,0xb61fb9a8,0x38720000,0x3bd93b64,0xb8720000,0xbbd9bb64,
0x39a83c00,0xb9a80000,0x39a83c00,0xb9a80000,0xbaa70000,0xb23ebb64,0x3aa70000,0x323e3b64,0xb61fbc00,0x3b6439a8,0xb61fbc00,0x3b6439a8,0x3bd90000,0xbaa7361f,0xbbd90000,0x3aa7b61f,
0x3c00,0xbc00,0x3c00,0xbc00,0xbbd90000,0x3aa7361f,0x3bd90000,0xbaa7b61f,0x361fbc00,0xbb6439a8,0x361fbc00,0xbb6439a8,0x3aa70000,0x323ebb64,0xbaa70000,0xb23e3b64,
0xb9a83c00,0x39a80000,0xb9a83c00,0x39a80000,0xb8720000,0xbbd93b64,0x38720000,0x3bd9bb64,0x3b64bc00,0x361fb9a8,0x3b64bc00,0x361fb9a8,0x323e0000,0x3872b61f,0xb23e0000,0xb872361f,
0x3c003c00,0x3c003c00,0x3c003c00,0x3c003c00,0xbaa7b9a8,0xbbd9bb64,0x3aa739a8,0x3bd93b64,0x361f0000,0x3b6439a8,0x361f0000,0x3b6439a8,0x323e39a8,0xbaa7b61f,0xb23eb9a8,0x3aa7361f,
0xb9a8bc00,0x39a80000,0xb9a8bc00,0x39a80000,0x3bd939a8,0xb872361f,0xbbd9b9a8,0x3872b61f,0xbb640000,0x361fb9a8,0xbb640000,0x361fb9a8,0x3872b9a8,0xb23e3b64,0xb87239a8,0x323ebb64,
0x3c00,0xbc00,0x3c00,0xbc00,0xb872b9a8,0x323e3b64,0x387239a8,0xb23ebb64,0x3b640000,0xb61fb9a8,0x3b640000,0xb61fb9a8,0xbbd939a8,0x3872361f,0x3bd9b9a8,0xb872b61f,
0x39a8bc00,0xb9a80000,0x39a8bc00,0xb9a80000,0xb23e39a8,0x3aa7b61f,0x323eb9a8,0xbaa7361f,0xb61f0000,0xbb6439a8,0xb61f0000,0xbb6439a8,0x3aa7b9a8,0x3bd9bb64,0xbaa739a8,0xbbd93b64,
0xbc003c00,0xbc003c00,0xbc003c00,0xbc003c00,0xbbd93c00,0xbaa73b64,0x3bd9bc00,0x3aa7bb64,0xbb643c00,0xb61f39a8,0xbb643c00,0xb61f39a8,0xbaa73c00,0x323e361f,0x3aa7bc00,0xb23eb61f,
0xb9a83c00,0x39a80000,0xb9a83c00,0x39a80000,0xb8723c00,0x3bd9b61f,0x3872bc00,0xbbd9361f,0xb61f3c00,0x3b64b9a8,0xb61f3c00,0x3b64b9a8,0xb23e3c00,0x3872bb64,0x323ebc00,0xb8723b64,
0x3c00,0xbc00,0x3c00,0xbc00,0x323e3c00,0xb872bb64,0xb23ebc00,0x38723b64,0x361f3c00,0xbb64b9a8,0x361f3c00,0xbb64b9a8,0x38723c00,0xbbd9b61f,0xb872bc00,0x3bd9361f,
0x39a83c00,0xb9a80000,0x39a83c00,0xb9a80000,0x3aa73c00,0xb23e361f,0xbaa7bc00,0x323eb61f,0x3b643c00,0x361f39a8,0x3b643c00,0x361f39a8,0x3bd93c00,0x3aa73b64,0xbbd9bc00,0xbaa7bb64,
0xbc003c00,0xbc003c00,0xbc003c00,0xbc003c00,0xb87239a8,0xb23e361f,0x3872b9a8,0x323eb61f,0x361f0000,0x3b64b9a8,0x361f0000,0x3b64b9a8,0x3bd9b9a8,0x3872bb64,0xbbd939a8,0xb8723b64,
0x39a8bc00,0xb9a80000,0x39a8bc00,0xb9a80000,0xb23eb9a8,0xbaa73b64,0x323e39a8,0x3aa7bb64,0xbb640000,0x361f39a8,0xbb640000,0x361f39a8,0xbaa739a8,0x3bd9b61f,0x3aa7b9a8,0xbbd9361f,
0x3c00,0xbc00,0x3c00,0xbc00,0x3aa739a8,0xbbd9b61f,0xbaa7b9a8,0x3bd9361f,0x3b640000,0xb61f39a8,0x3b640000,0xb61f39a8,0x323eb9a8,0x3aa73b64,0xb23e39a8,0xbaa7bb64,
0xb9a8bc00,0x39a80000,0xb9a8bc00,0x39a80000,0xbbd9b9a8,0xb872bb64,0x3bd939a8,0x38723b64,0xb61f0000,0xbb64b9a8,0xb61f0000,0xbb64b9a8,0x387239a8,0x323e361f,0xb872b9a8,0xb23eb61f,
0xbc003c00,0xbc003c00,0xbc003c00,0xbc003c00,0x323e0000,0x3872b61f,0xb23e0000,0xb872361f,0x3b64bc00,0x361fb9a8,0x3b64bc00,0x361fb9a8,0xb8720000,0xbbd93b64,0x38720000,0x3bd9bb64,
0xb9a83c00,0x39a80000,0xb9a83c00,0x39a80000,0x3aa70000,0x323ebb64,0xbaa70000,0xb23e3b64,0x361fbc00,0xbb6439a8,0x361fbc00,0xbb6439a8,0xbbd90000,0x3aa7361f,0x3bd90000,0xbaa7b61f,
0x3c00,0xbc00,0x3c00,0xbc00,0x3bd90000,0xbaa7361f,0xbbd90000,0x3aa7b61f,0xb61fbc00,0x3b6439a8,0xb61fbc00,0x3b6439a8,0xbaa70000,0xb23ebb64,0x3aa70000,0x323e3b64,
0x39a83c00,0xb9a80000,0x39a83c00,0xb9a80000,0x38720000,0x3bd93b64,0xb8720000,0xbbd9bb64,0xbb64bc00,0xb61fb9a8,0xbb64bc00,0xb61fb9a8,0xb23e0000,0xb872b61f,0x323e0000,0x3872361f,
0xbc003c00,0xbc003c00,0xbc003c00,0xbc003c00,0x3aa7b9a8,0x3bd9bb64,0xbaa739a8,0xbbd93b64,0xb61f0000,0xbb6439a8,0xb61f0000,0xbb6439a8,0xb23e39a8,0x3aa7b61f,0x323eb9a8,0xbaa7361f,
0x39a8bc00,0xb9a80000,0x39a8bc00,0xb9a80000,0xbbd939a8,0x3872361f,0x3bd9b9a8,0xb872b61f,0x3b640000,0xb61fb9a8,0x3b640000,0xb61fb9a8,0xb872b9a8,0x323e3b64,0x387239a8,0xb23ebb64,
0x3c00,0xbc00,0x3c00,0xbc00,0x3872b9a8,0xb23e3b64,0xb87239a8,0x323ebb64,0xbb640000,0x361fb9a8,0xbb640000,0x361fb9a8,0x3bd939a8,0xb872361f,0xbbd9b9a8,0x3872b61f,
0xb9a8bc00,0x39a80000,0xb9a8bc00,0x39a80000,0x323e39a8,0xbaa7b61f,0xb23eb9a8,0x3aa7361f,0x361f0000,0x3b6439a8,0x361f0000,0x3b6439a8,0xbaa7b9a8,0xbbd9bb64,0x3aa739a8,0x3bd93b64,
0x0,0x0,0x0,0x0,0x323e0000,0x3872361f,0xb23e0000,0xb872b61f,0x361f0000,0x3b6439a8,0x361f0000,0x3b6439a8,0x38720000,0x3bd93b64,0xb8720000,0xbbd9bb64,
0x39a80000,0x39a83c00,0x39a80000,0x39a83c00,0x3aa70000,0x323e3b64,0xbaa70000,0xb23ebb64,0x3b640000,0xb61f39a8,0x3b640000,0xb61f39a8,0x3bd90000,0xbaa7361f,0xbbd90000,0x3aa7b61f,
0x3c000000,0xbc000000,0x3c000000,0xbc000000,0x3bd90000,0xbaa7b61f,0xbbd90000,0x3aa7361f,0x3b640000,0xb61fb9a8,0x3b640000,0xb61fb9a8,0x3aa70000,0x323ebb64,0xbaa70000,0xb23e3b64,
0x39a80000,0x39a8bc00,0x39a80000,0x39a8bc00,0x38720000,0x3bd9bb64,0xb8720000,0xbbd93b64,0x361f0000,0x3b64b9a8,0x361f0000,0x3b64b9a8,0x323e0000,0x3872b61f,0xb23e0000,0xb872361f,
0x0,0x0,0x0,0x0,0x3aa739a8,0x3bd93b64,0xbaa7b9a8,0xbbd9bb64,0x3b643c00,0x361f39a8,0x3b643c00,0x361f39a8,0x323e39a8,0xbaa7b61f,0xb23eb9a8,0x3aa7361f,
0xb9a80000,0xb9a8bc00,0xb9a80000,0xb9a8bc00,0xbbd9b9a8,0x3872b61f,0x3bd939a8,0xb872361f,0xb61fbc00,0x3b6439a8,0xb61fbc00,0x3b6439a8,0x3872b9a8,0xb23e3b64,0xb87239a8,0x323ebb64,
0x3c000000,0xbc000000,0x3c000000,0xbc000000,0x387239a8,0xb23ebb64,0xb872b9a8,0x323e3b64,0xb61f3c00,0x3b64b9a8,0xb61f3c00,0x3b64b9a8,0xbbd939a8,0x3872361f,0x3bd9b9a8,0xb872b61f,
0xb9a80000,0xb9a83c00,0xb9a80000,0xb9a83c00,0x323eb9a8,0xbaa7361f,0xb23e39a8,0x3aa7b61f,0x3b64bc00,0x361fb9a8,0x3b64bc00,0x361fb9a8,0x3aa7b9a8,0x3bd9bb64,0xbaa739a8,0xbbd93b64,
0x0,0x0,0x0,0x0,0x3bd93c00,0x3aa73b64,0xbbd9bc00,0xbaa7bb64,0xb61f0000,0xbb64b9a8,0xb61f0000,0xbb64b9a8,0xbaa7bc00,0x323eb61f,0x3aa73c00,0xb23e361f,
0x39a80000,0x39a83c00,0x39a80000,0x39a83c00,0x38723c00,0xbbd9b61f,0xb872bc00,0x3bd9361f,0xbb640000,0x361fb9a8,0xbb640000,0x361fb9a8,0xb23ebc00,0x38723b64,0x323e3c00,0xb872bb64,
0x3c000000,0xbc000000,0x3c000000,0xbc000000,0xb23e3c00,0x3872bb64,0x323ebc00,0xb8723b64,0xbb640000,0x361f39a8,0xbb640000,0x361f39a8,0x3872bc00,0xbbd9361f,0xb8723c00,0x3bd9b61f,
0x39a80000,0x39a8bc00,0x39a80000,0x39a8bc00,0xbaa73c00,0x323e361f,0x3aa7bc00,0xb23eb61f,0xb61f0000,0xbb6439a8,0xb61f0000,0xbb6439a8,0x3bd9bc00,0x3aa7bb64,0xbbd93c00,0xbaa73b64,
0x0,0x0,0x0,0x0,0x387239a8,0x323e361f,0xb872b9a8,0xb23eb61f,0xbb64bc00,0xb61fb9a8,0xbb64bc00,0xb61fb9a8,0x3bd939a8,0x38723b64,0xbbd9b9a8,0xb872bb64,
0xb9a80000,0xb9a8bc00,0xb9a80000,0xb9a8bc00,0x323eb9a8,0x3aa73b64,0xb23e39a8,0xbaa7bb64,0x361f3c00,0xbb64b9a8,0x361f3c00,0xbb64b9a8,0xbaa7b9a8,0x3bd9361f,0x3aa739a8,0xbbd9b61f,
0x3c000000,0xbc000000,0x3c000000,0xbc000000,0xbaa739a8,0x3bd9b61f,0x3aa7b9a8,0xbbd9361f,0x361fbc00,0xbb6439a8,0x361fbc00,0xbb6439a8,0x323e39a8,0x3aa7bb64,0xb23eb9a8,0xbaa73b64,
0xb9a80000,0xb9a83c00,0xb9a80000,0xb9a83c00,0x3bd9b9a8,0x3872bb64,0xbbd939a8,0xb8723b64,0xbb643c00,0xb61f39a8,0xbb643c00,0xb61f39a8,0x3872b9a8,0x323eb61f,0xb87239a8,0xb23e361f,
0x0,0x0,0x0,0x0,0xb23e0000,0xb872361f,0x323e0000,0x3872b61f,0xb61f0000,0xbb6439a8,0xb61f0000,0xbb6439a8,0xb8720000,0xbbd93b64,0x38720000,0x3bd9bb64,
0xb9a80000,0xb9a83c00,0xb9a80000,0xb9a83c00,0xbaa70000,0xb23e3b64,0x3aa70000,0x323ebb64,0xbb640000,0x361f39a8,0xbb640000,0x361f39a8,0xbbd90000,0x3aa7361f,0x3bd90000,0xbaa7b61f,
0xbc000000,0x3c000000,0xbc000000,0x3c000000,0xbbd90000,0x3aa7b61f,0x3bd90000,0xbaa7361f,0xbb640000,0x361fb9a8,0xbb640000,0x361fb9a8,0xbaa70000,0xb23ebb64,0x3aa70000,0x323e3b64,
0xb9a80000,0xb9a8bc00,0xb9a80000,0xb9a8bc00,0xb8720000,0xbbd9bb64,0x38720000,0x3bd93b64,0xb61f0000,0xbb64b9a8,0xb61f0000,0xbb64b9a8,0xb23e0000,0xb872b61f,0x323e0000,0x3872361f,
0x0,0x0,0x0,0x0,0xbaa739a8,0xbbd93b64,0x3aa7b9a8,0x3bd9bb64,0xbb643c00,0xb61f39a8,0xbb643c00,0xb61f39a8,0xb23e39a8,0x3aa7b61f,0x323eb9a8,0xbaa7361f,
0x39a80000,0x39a8bc00,0x39a80000,0x39a8bc00,0x3bd9b9a8,0xb872b61f,0xbbd939a8,0x3872361f,0x361fbc00,0xbb6439a8,0x361fbc00,0xbb6439a8,0xb872b9a8,0x323e3b64,0x387239a8,0xb23ebb64,
0xbc000000,0x3c000000,0xbc000000,0x3c000000,0xb87239a8,0x323ebb64,0x3872b9a8,0xb23e3b64,0x361f3c00,0xbb64b9a8,0x361f3c00,0xbb64b9a8,0x3bd939a8,0xb872361f,0xbbd9b9a8,0x3872b61f,
0x39a80000,0x39a83c00,0x39a80000,0x39a83c00,0xb23eb9a8,0x3aa7361f,0x323e39a8,0xbaa7b61f,0xbb64bc00,0xb61fb9a8,0xbb64bc00,0xb61fb9a8,0xbaa7b9a8,0xbbd9bb64,0x3aa739a8,0x3bd93b64,
0x0,0x0,0x0,0x0,0xbbd93c00,0xbaa73b64,0x3bd9bc00,0x3aa7bb64,0x361f0000,0x3b64b9a8,0x361f0000,0x3b64b9a8,0x3aa7bc00,0xb23eb61f,0xbaa73c00,0x323e361f,
0xb9a80000,0xb9a83c00,0xb9a80000,0xb9a83c00,0xb8723c00,0x3bd9b61f,0x3872bc00,0xbbd9361f,0x3b640000,0xb61fb9a8,0x3b640000,0xb61fb9a8,0x323ebc00,0xb8723b64,0xb23e3c00,0x3872bb64,
0xbc000000,0x3c000000,0xbc000000,0x3c000000,0x323e3c00,0xb872bb64,0xb23ebc00,0x38723b64,0x3b640000,0xb61f39a8,0x3b640000,0xb61f39a8,0xb872bc00,0x3bd9361f,0x38723c00,0xbbd9b61f,
0xb9a80000,0xb9a8bc00,0xb9a80000,0xb9a8bc00,0x3aa73c00,0xb23e361f,0xbaa7bc00,0x323eb61f,0x361f0000,0x3b6439a8,0x361f0000,0x3b6439a8,0xbbd9bc00,0xbaa7bb64,0x3bd93c00,0x3aa73b64,
0x0,0x0,0x0,0x0,0xb87239a8,0xb23e361f,0x3872b9a8,0x323eb61f,0x3b64bc00,0x361fb9a8,0x3b64bc00,0x361fb9a8,0xbbd939a8,0xb8723b64,0x3bd9b9a8,0x3872bb64,
0x39a80000,0x39a8bc00,0x39a80000,0x39a8bc00,0xb23eb9a8,0xbaa73b64,0x323e39a8,0x3aa7bb64,0xb61f3c00,0x3b64b9a8,0xb61f3c00,0x3b64b9a8,0x3aa7b9a8,0xbbd9361f,0xbaa739a8,0x3bd9b61f,
0xbc000000,0x3c000000,0xbc000000,0x3c000000,0x3aa739a8,0xbbd9b61f,0xbaa7b9a8,0x3bd9361f,0xb61fbc00,0x3b6439a8,0xb61fbc00,0x3b6439a8,0xb23e39a8,0xbaa7bb64,0x323eb9a8,0x3aa73b64,
0x39a80000,0x39a83c00,0x39a80000,0x39a83c00,0xbbd9b9a8,0xb872bb64,0x3bd939a8,0x38723b64,0x3b643c00,0x361f39a8,0x3b643c00,0x361f39a8,0xb872b9a8,0xb23eb61f,0x387239a8,0x323e361f,
0x3bf63c00,0x3ba83bd9,0xae460000,0xb4a5b23e,0x3ba83c00,0x39133aa7,0x34a50000,0x3a2f3872,0x3b0e3c00,0x2e463872,0xb78b0000,0xbbf6baa7,0x3a2f3c00,0xb78b323e,0x39130000,0x3b0e3bd9,
0x39133c00,0xbb0eb23e,0xba2f0000,0xb78bbbd9,0x378b3c00,0xbbf6b872,0x3b0e0000,0xae463aa7,0x34a53c00,0xba2fbaa7,0xbba80000,0x3913b872,0x2e463c00,0xb4a5bbd9,0x3bf60000,0xbba8323e,
0xae463c00,0x34a5bbd9,0xbbf60000,0x3ba8323e,0xb4a53c00,0x3a2fbaa7,0x3ba80000,0xb913b872,0xb78b3c00,0x3bf6b872,0xbb0e0000,0x2e463aa7,0xb9133c00,0x3b0eb23e,0x3a2f0000,0x378bbbd9,
0xba2f3c00,0x378b323e,0xb9130000,0xbb0e3bd9,0xbb0e3c00,0xae463872,0x378b0000,0x3bf6baa7,0xbba83c00,0xb9133aa7,0xb4a50000,0xba2f3872,0xbbf63c00,0xbba83bd9,0x2e460000,0x34a5b23e,
0x3b0e3b64,0x3a2f3aa7,0xb78bb61f,0xb913b872,0x2e46361f,0xb78bb23e,0x3bf63b64,0x3b0e3bd9,0xba2fb61f,0xbba8bbd9,0xb913bb64,0x34a5b23e,0xbba8bb64,0x2e46b872,0xb4a5361f,0xbbf6baa7,
0xb4a5bb64,0x3bf63872,0x3ba8361f,0x2e463aa7,0x3913b61f,0x34a53bd9,0xba2fbb64,0x3ba8323e,0x3bf6361f,0xbb0e323e,0xae463b64,0xb78bbbd9,0x378b3b64,0xb913baa7,0x3b0eb61f,0xba2f3872,
0xb78b3b64,0x3913baa7,0xbb0eb61f,0x3a2f3872,0xbbf6361f,0x3b0e323e,0x2e463b64,0x378bbbd9,0xb913b61f,0xb4a53bd9,0x3a2fbb64,0xbba8323e,0x34a5bb64,0xbbf63872,0xbba8361f,0xae463aa7,
0x3ba8bb64,0xae46b872,0x34a5361f,0x3bf6baa7,0x3a2fb61f,0x3ba8bbd9,0x3913bb64,0xb4a5b23e,0xae46361f,0x378bb23e,0xbbf63b64,0xbb0e3bd9,0xbb0e3b64,0xba2f3aa7,0x378bb61f,0x3913b872,
0x391339a8,0x378b3872,0xba2fb9a8,0xbb0ebaa7,0xbb0eb9a8,0xbbf6bbd9,0x378b39a8,0xae46323e,0xb4a5b9a8,0x3913323e,0x3ba839a8,0x3a2f3bd9,0x3bf639a8,0x34a53aa7,0xae46b9a8,0x3ba83872,
0xae4639a8,0xbba8baa7,0xbbf6b9a8,0x34a5b872,0xbba8b9a8,0x3a2fb23e,0xb4a539a8,0xb913bbd9,0x378bb9a8,0x2e463bd9,0x3b0e39a8,0xbbf6b23e,0x3a2f39a8,0xbb0eb872,0x3913b9a8,0xb78b3aa7,
0xba2f39a8,0x3b0eb872,0xb913b9a8,0x378b3aa7,0xb78bb9a8,0xae463bd9,0xbb0e39a8,0x3bf6b23e,0x3ba8b9a8,0xba2fb23e,0x34a539a8,0x3913bbd9,0x2e4639a8,0x3ba8baa7,0x3bf6b9a8,0xb4a5b872,
0xbbf639a8,0xb4a53aa7,0x2e46b9a8,0xbba83872,0x34a5b9a8,0xb913323e,0xbba839a8,0xba2f3bd9,0x3b0eb9a8,0x3bf6bbd9,0xb78b39a8,0x2e46323e,0xb91339a8,0xb78b3872,0x3a2fb9a8,0x3b0ebaa7,
0x34a5361f,0x2e46323e,0xbba8bb64,0xbbf6bbd9,0xba2fbb64,0xb4a5b872,0xb913b61f,0xbba8baa7,0x3bf63b64,0x378b3aa7,0xae46361f,0xbb0eb872,0xbb0eb61f,0xb913bbd9,0x378b3b64,0xba2fb23e,
0x378bb61f,0x3a2f3bd9,0x3b0e3b64,0xb913323e,0x2e463b64,0xbb0ebaa7,0x3bf6361f,0xb78b3872,0xb913bb64,0x3ba83872,0x3a2fb61f,0xb4a53aa7,0x3ba8361f,0xbbf6b23e,0x34a5bb64,0xae463bd9,
0xbba8361f,0x3bf6b23e,0xb4a5bb64,0x2e463bd9,0x3913bb64,0xbba83872,0xba2fb61f,0x34a53aa7,0xae463b64,0x3b0ebaa7,0xbbf6361f,0x378b3872,0xb78bb61f,0xba2f3bd9,0xbb0e3b64,0x3913323e,
0x3b0eb61f,0x3913bbd9,0xb78b3b64,0x3a2fb23e,0xbbf63b64,0xb78b3aa7,0x2e46361f,0x3b0eb872,0x3a2fbb64,0x34a5b872,0x3913b61f,0x3ba8baa7,0xb4a5361f,0xae46323e,0x3ba8bb64,0x3bf6bbd9,
0xae468000,0xb4a5b23e,0xbbf6bc00,0xbba8bbd9,0xb4a58000,0xba2fb872,0x3ba83c00,0x39133aa7,0xb78b8000,0xbbf6baa7,0xbb0ebc00,0xae46b872,0xb9138000,0xbb0ebbd9,0x3a2f3c00,0xb78b323e,
0xba2f8000,0xb78bbbd9,0xb913bc00,0x3b0e323e,0xbb0e8000,0x2e46baa7,0x378b3c00,0xbbf6b872,0xbba88000,0x3913b872,0xb4a5bc00,0x3a2f3aa7,0xbbf68000,0x3ba8b23e,0x2e463c00,0xb4a5bbd9,
0xbbf68000,0x3ba8323e,0x2e46bc00,0xb4a53bd9,0xbba88000,0x39133872,0xb4a53c00,0x3a2fbaa7,0xbb0e8000,0x2e463aa7,0x378bbc00,0xbbf63872,0xba2f8000,0xb78b3bd9,0xb9133c00,0x3b0eb23e,
0xb9138000,0xbb0e3bd9,0x3a2fbc00,0xb78bb23e,0xb78b8000,0xbbf63aa7,0xbb0e3c00,0xae463872,0xb4a58000,0xba2f3872,0x3ba8bc00,0x3913baa7,0xae468000,0xb4a5323e,0xbbf63c00,0xbba83bd9,
0xb78bb61f,0xb913b872,0xbb0ebb64,0xba2fbaa7,0xbbf6bb64,0xbb0ebbd9,0x2e46361f,0xb78bb23e,0xb913bb64,0x34a5b23e,0x3a2f361f,0x3ba83bd9,0x34a5b61f,0x3bf63aa7,0xbba8bb64,0x2e46b872,
0x3ba8361f,0x2e463aa7,0x34a53b64,0xbbf6b872,0x3a2f3b64,0xbba8b23e,0x3913b61f,0x34a53bd9,0xae463b64,0xb78bbbd9,0xbbf6b61f,0x3b0eb23e,0xbb0e361f,0x3a2fb872,0x378b3b64,0xb913baa7,
0xbb0eb61f,0x3a2f3872,0x378bbb64,0xb9133aa7,0xae46bb64,0xb78b3bd9,0xbbf6361f,0x3b0e323e,0x3a2fbb64,0xbba8323e,0x3913361f,0x34a5bbd9,0x3ba8b61f,0x2e46baa7,0x34a5bb64,0xbbf63872,
0x34a5361f,0x3bf6baa7,0xbba83b64,0x2e463872,0xb9133b64,0x34a5323e,0x3a2fb61f,0x3ba8bbd9,0xbbf63b64,0xbb0e3bd9,0x2e46b61f,0xb78b323e,0xb78b361f,0xb9133872,0xbb0e3b64,0xba2f3aa7,
0xba2fb9a8,0xbb0ebaa7,0xb913b9a8,0xb78bb872,0xb78bb9a8,0x2e46b23e,0xbb0eb9a8,0xbbf6bbd9,0x3ba839a8,0x3a2f3bd9,0x34a539a8,0xb913b23e,0x2e4639a8,0xbba8b872,0x3bf639a8,0x34a53aa7,
0xbbf6b9a8,0x34a5b872,0x2e46b9a8,0x3ba83aa7,0x34a5b9a8,0x39133bd9,0xbba8b9a8,0x3a2fb23e,0x3b0e39a8,0xbbf6b23e,0xb78b39a8,0xae46bbd9,0xb91339a8,0x378bbaa7,0x3a2f39a8,0xbb0eb872,
0xb913b9a8,0x378b3aa7,0x3a2fb9a8,0xbb0e3872,0x3b0eb9a8,0xbbf6323e,0xb78bb9a8,0xae463bd9,0x34a539a8,0x3913bbd9,0xbba839a8,0x3a2f323e,0xbbf639a8,0x34a53872,0x2e4639a8,0x3ba8baa7,
0x2e46b9a8,0xbba83872,0x3bf6b9a8,0x34a5baa7,0x3ba8b9a8,0x3a2fbbd9,0x34a5b9a8,0xb913323e,0xb78b39a8,0x2e46323e,0xbb0e39a8,0xbbf63bd9,0xba2f39a8,0xbb0e3aa7,0xb91339a8,0xb78b3872,
0xbba8bb64,0xbbf6bbd9,0xb4a5b61f,0xae46b23e,0x3913361f,0x3ba83aa7,0xba2fbb64,0xb4a5b872,0xae46361f,0xbb0eb872,0xbbf6bb64,0xb78bbaa7,0xb78bbb64,0x3a2f323e,0xbb0eb61f,0xb913bbd9,
0x3b0e3b64,0xb913323e,0xb78b361f,0xba2fbbd9,0xbbf6b61f,0x378bb872,0x2e463b64,0xbb0ebaa7,0x3a2fb61f,0xb4a53aa7,0x39133b64,0xbba8b872,0xb4a53b64,0x2e46bbd9,0x3ba8361f,0xbbf6b23e,
0xb4a5bb64,0x2e463bd9,0x3ba8b61f,0xbbf6323e,0x3a2f361f,0xb4a5baa7,0x3913bb64,0xbba83872,0xbbf6361f,0x378b3872,0x2e46bb64,0xbb0e3aa7,0x3b0ebb64,0xb913b23e,0xb78bb61f,0xba2f3bd9,
0xb78b3b64,0x3a2fb23e,0xbb0e361f,0xb9133bd9,0xae46b61f,0xbb0e3872,0xbbf63b64,0xb78b3aa7,0x3913b61f,0x3ba8baa7,0xba2f3b64,0xb4a53872,0xbba83b64,0xbbf63bd9,0xb4a5361f,0xae46323e,
0x3c00,0x3c00,0x3c00,0x3c00,0x3c008000,0x3c008000,0x3c008000,0x3c008000,0x3c00,0x361f3b64,0x39a839a8,0x3b64361f,0x3c008000,0x3b64b61f,0x39a8b9a8,0x361fbb64,
0x3c00,0x39a839a8,0x3c000000,0x39a8b9a8,0x3c008000,0x39a8b9a8,0xbc00,0xb9a8b9a8,0x3c00,0x3b64361f,0x39a8b9a8,0xb61fbb64,0x3c008000,0x361fbb64,0xb9a8b9a8,0xbb64361f,
0x3c00,0x3c000000,0xbc00,0xbc000000,0x3c008000,0xbc00,0xbc008000,0x3c00,0x3c00,0x3b64b61f,0xb9a8b9a8,0xb61f3b64,0x3c008000,0xb61fbb64,0xb9a839a8,0x3b64361f,
0x3c00,0x39a8b9a8,0xbc000000,0x39a839a8,0x3c008000,0xb9a8b9a8,0x3c00,0x39a8b9a8,0x3c00,0x361fbb64,0xb9a839a8,0x3b64b61f,0x3c008000,0xbb64b61f,0x39a839a8,0xb61fbb64,
0x3c00,0x3c00,0x3c00,0x3c00,0x3c008000,0x3c008000,0x3c008000,0x3c008000,0x3c000000,0x3b64b61f,0x39a8b9a8,0x361fbb64,0xbc00,0xb61fbb64,0xb9a8b9a8,0xbb64b61f,
0xbc00,0xb9a8b9a8,0xbc000000,0xb9a839a8,0xbc008000,0xb9a839a8,0x3c00,0x39a839a8,0xbc000000,0xb61f3b64,0x39a839a8,0x3b64b61f,0x3c00,0x3b64361f,0x39a8b9a8,0xb61fbb64,
0x3c00,0x3c000000,0xbc00,0xbc000000,0x3c008000,0xbc00,0xbc008000,0x3c00,0x3c000000,0xb61fbb64,0xb9a839a8,0x3b64361f,0xbc00,0xbb64361f,0x39a839a8,0x361fbb64,
0xbc00,0xb9a839a8,0x3c000000,0xb9a8b9a8,0xbc008000,0x39a839a8,0xbc00,0xb9a839a8,0xbc000000,0x3b64361f,0xb9a8b9a8,0x361f3b64,0x3c00,0x361fbb64,0xb9a839a8,0x3b64b61f,
0x3c00,0x3c00,0x3c00,0x3c00,0x3c008000,0x3c008000,0x3c008000,0x3c008000,0xbc00,0xb61fbb64,0xb9a8b9a8,0xbb64b61f,0xbc008000,0xbb64361f,0xb9a839a8,0xb61f3b64,
0x3c00,0x39a839a8,0x3c000000,0x39a8b9a8,0x3c008000,0x39a8b9a8,0xbc00,0xb9a8b9a8,0xbc00,0xbb64b61f,0xb9a839a8,0x361f3b64,0xbc008000,0xb61f3b64,0x39a839a8,0x3b64b61f,
0x3c00,0x3c000000,0xbc00,0xbc000000,0x3c008000,0xbc00,0xbc008000,0x3c00,0xbc00,0xbb64361f,0x39a839a8,0x361fbb64,0xbc008000,0x361f3b64,0x39a8b9a8,0xbb64b61f,
0x3c00,0x39a8b9a8,0xbc000000,0x39a839a8,0x3c008000,0xb9a8b9a8,0x3c00,0x39a8b9a8,0xbc00,0xb61f3b64,0x39a8b9a8,0xbb64361f,0xbc008000,0x3b64361f,0xb9a8b9a8,0x361f3b64,
0x3c00,0x3c00,0x3c00,0x3c00,0x3c008000,0x3c008000,0x3c008000,0x3c008000,0xbc000000,0xbb64361f,0xb9a839a8,0xb61f3b64,0x3c00,0x361f3b64,0x39a839a8,0x3b64361f,
0xbc00,0xb9a8b9a8,0xbc000000,0xb9a839a8,0xbc008000,0xb9a839a8,0x3c00,0x39a839a8,0x3c000000,0x361fbb64,0xb9a8b9a8,0xbb64361f,0xbc00,0xbb64b61f,0xb9a839a8,0x361f3b64,
0x3c00,0x3c000000,0xbc00,0xbc000000,0x3c008000,0xbc00,0xbc008000,0x3c00,0xbc000000,0x361f3b64,0x39a8b9a8,0xbb64b61f,0x3c00,0x3b64b61f,0xb9a8b9a8,0xb61f3b64,
0xbc00,0xb9a839a8,0x3c000000,0xb9a8b9a8,0xbc008000,0x39a839a8,0xbc00,0xb9a839a8,0x3c000000,0xbb64b61f,0x39a839a8,0xb61fbb64,0xbc00,0xb61f3b64,0x39a8b9a8,0xbb64361f,
0x3c00,0xbc00,0x3c00,0xbc00,0x3c008000,0xbc008000,0x3c008000,0xbc008000,0x3c00,0xb61fbb64,0x39a839a8,0xbb64b61f,0x3c008000,0xbb64361f,0x39a8b9a8,0xb61f3b64,
0x3c00,0xb9a8b9a8,0x3c000000,0xb9a839a8,0x3c008000,0xb9a839a8,0xbc00,0x39a839a8,0x3c00,0xbb64b61f,0x39a8b9a8,0x361f3b64,0x3c008000,0xb61f3b64,0xb9a8b9a8,0x3b64b61f,
0x3c00,0xbc000000,0xbc00,0x3c000000,0x3c008000,0x3c00,0xbc008000,0xbc00,0x3c00,0xbb64361f,0xb9a8b9a8,0x361fbb64,0x3c008000,0x361f3b64,0xb9a839a8,0xbb64b61f,
0x3c00,0xb9a839a8,0xbc000000,0xb9a8b9a8,0x3c008000,0x39a839a8,0x3c00,0xb9a839a8,0x3c00,0xb61f3b64,0xb9a839a8,0xbb64361f,0x3c008000,0x3b64361f,0x39a839a8,0x361f3b64,
0x3c00,0xbc00,0x3c00,0xbc00,0x3c008000,0xbc008000,0x3c008000,0xbc008000,0x3c000000,0xbb64361f,0x39a8b9a8,0xb61f3b64,0xbc00,0x361f3b64,0xb9a8b9a8,0x3b64361f,
0xbc00,0x39a839a8,0xbc000000,0x39a8b9a8,0xbc008000,0x39a8b9a8,0x3c00,0xb9a8b9a8,0xbc000000,0x361fbb64,0x39a839a8,0xbb64361f,0x3c00,0xbb64b61f,0x39a8b9a8,0x361f3b64,
0x3c00,0xbc000000,0xbc00,0x3c000000,0x3c008000,0x3c00,0xbc008000,0xbc00,0x3c000000,0x361f3b64,0xb9a839a8,0xbb64b61f,0xbc00,0x3b64b61f,0x39a839a8,0xb61f3b64,
0xbc00,0x39a8b9a8,0x3c000000,0x39a839a8,0xbc008000,0xb9a8b9a8,0xbc00,0x39a8b9a8,0xbc000000,0xbb64b61f,0xb9a8b9a8,0xb61fbb64,0x3c00,0xb61f3b64,0xb9a839a8,0xbb64361f,
0x3c00,0xbc00,0x3c00,0xbc00,0x3c008000,0xbc008000,0x3c008000,0xbc008000,0xbc00,0x361f3b64,0xb9a8b9a8,0x3b64361f,0xbc008000,0x3b64b61f,0xb9a839a8,0x361fbb64,
0x3c00,0xb9a8b9a8,0x3c000000,0xb9a839a8,0x3c008000,0xb9a839a8,0xbc00,0x39a839a8,0xbc00,0x3b64361f,0xb9a839a8,0xb61fbb64,0xbc008000,0x361fbb64,0x39a839a8,0xbb64361f,
0x3c00,0xbc000000,0xbc00,0x3c000000,0x3c008000,0x3c00,0xbc008000,0xbc00,0xbc00,0x3b64b61f,0x39a839a8,0xb61f3b64,0xbc008000,0xb61fbb64,0x39a8b9a8,0x3b64361f,
0x3c00,0xb9a839a8,0xbc000000,0xb9a8b9a8,0x3c008000,0x39a839a8,0x3c00,0xb9a839a8,0xbc00,0x361fbb64,0x39a8b9a8,0x3b64b61f,0xbc008000,0xbb64b61f,0xb9a8b9a8,0xb61fbb64,
0x3c00,0xbc00,0x3c00,0xbc00,0x3c008000,0xbc008000,0x3c008000,0xbc008000,0xbc000000,0x3b64b61f,0xb9a839a8,0x361fbb64,0x3c00,0xb61fbb64,0x39a839a8,0xbb64b61f,
0xbc00,0x39a839a8,0xbc000000,0x39a8b9a8,0xbc008000,0x39a8b9a8,0x3c00,0xb9a8b9a8,0x3c000000,0xb61f3b64,0xb9a8b9a8,0x3b64b61f,0xbc00,0x3b64361f,0xb9a839a8,0xb61fbb64,
0x3c00,0xbc000000,0xbc00,0x3c000000,0x3c008000,0x3c00,0xbc008000,0xbc00,0xbc000000,0xb61fbb64,0x39a8b9a8,0x3b64361f,0x3c00,0xbb64361f,0xb9a8b9a8,0x361fbb64,
0xbc00,0x39a8b9a8,0x3c000000,0x39a839a8,0xbc008000,0xb9a8b9a8,0xbc00,0x39a8b9a8,0x3c000000,0x3b64361f,0x39a839a8,0x361f3b64,0xbc00,0x361fbb64,0x39a8b9a8,0x3b64b61f,
0x80003f80,0x80003f80,0x80003f80,0x80003f80,0x3f800000,0x3f800000,0x3f800000,0x3f800000,0x80003f80,0xbec43f6d,0x8000bf80,0x3ec4bf6d,0x3f800000,0x3f6d3ec4,0xbf800000,0xbf6dbec4,
0x80003f80,0xbf353f35,0x80003f80,0xbf353f35,0x3f800000,0x3f353f35,0x3f800000,0x3f353f35,0x80003f80,0xbf6d3ec4,0x8000bf80,0x3f6dbec4,0x3f800000,0x3ec43f6d,0xbf800000,0xbec4bf6d,
0x80003f80,0xbf800000,0x80003f80,0xbf800000,0x3f800000,0x3f80,0x3f800000,0x3f80,0x80003f80,0xbf6dbec4,0x8000bf80,0x3f6d3ec4,0x3f800000,0xbec43f6d,0xbf800000,0x3ec4bf6d,
0x80003f80,0xbf35bf35,0x80003f80,0xbf35bf35,0x3f800000,0xbf353f35,0x3f800000,0xbf353f35,0x80003f80,0xbec4bf6d,0x8000bf80,0x3ec43f6d,0x3f800000,0xbf6d3ec4,0xbf800000,0x3f6dbec4,
0x80003f80,0x80003f80,0x80003f80,0x80003f80,0x3f800000,0x3f800000,0x3f800000,0x3f800000,0xbf353f35,0xbf6d3ec4,0x3f35bf35,0x3f6dbec4,0x3f353f35,0x3ec43f6d,0xbf35bf35,0xbec4bf6d,
0xbf800000,0xbf35bf35,0xbf800000,0xbf35bf35,0x3f80,0xbf353f35,0x3f80,0xbf353f35,0xbf35bf35,0x3ec4bf6d,0x3f353f35,0xbec43f6d,0xbf353f35,0xbf6dbec4,0x3f35bf35,0x3f6d3ec4,
0x8000bf80,0x3f800000,0x8000bf80,0x3f800000,0xbf800000,0xbf80,0xbf800000,0xbf80,0x3f35bf35,0x3ec43f6d,0xbf353f35,0xbec4bf6d,0xbf35bf35,0x3f6dbec4,0x3f353f35,0xbf6d3ec4,
0x3f800000,0xbf353f35,0x3f800000,0xbf353f35,0xbf80,0x3f353f35,0xbf80,0x3f353f35,0x3f353f35,0xbf6dbec4,0xbf35bf35,0x3f6d3ec4,0x3f35bf35,0xbec43f6d,0xbf353f35,0x3ec4bf6d,
0x80003f80,0x80003f80,0x80003f80,0x80003f80,0x3f800000,0x3f800000,0x3f800000,0x3f800000,0xbf800000,0xbf6dbec4,0x3f800000,0x3f6d3ec4,0x3f80,0xbec43f6d,0xbf80,0x3ec4bf6d,
0x8000bf80,0x3f35bf35,0x8000bf80,0x3f35bf35,0xbf800000,0xbf35bf35,0xbf800000,0xbf35bf35,0x3f800000,0x3ec43f6d,0xbf800000,0xbec4bf6d,0xbf80,0x3f6dbec4,0x3f80,0xbf6d3ec4,
0x80003f80,0xbf800000,0x80003f80,0xbf800000,0x3f800000,0x3f80,0x3f800000,0x3f80,0xbf800000,0x3ec4bf6d,0x3f800000,0xbec43f6d,0x3f80,0xbf6dbec4,0xbf80,0x3f6d3ec4,
0x8000bf80,0x3f353f35,0x8000bf80,0x3f353f35,0xbf800000,0x3f35bf35,0xbf800000,0x3f35bf35,0x3f800000,0xbf6d3ec4,0xbf800000,0x3f6dbec4,0xbf80,0x3ec43f6d,0x3f80,0xbec4bf6d,
0x80003f80,0x80003f80,0x80003f80,0x80003f80,0x3f800000,0x3f800000,0x3f800000,0x3f800000,0xbf35bf35,0xbec4bf6d,0x3f353f35,0x3ec43f6d,0xbf353f35,0xbf6d3ec4,0x3f35bf35,0x3f6dbec4,
0x3f800000,0x3f353f35,0x3f800000,0x3f353f35,0xbf80,0x3f35bf35,0xbf80,0x3f35bf35,0xbf353f35,0xbf6dbec4,0x3f35bf35,0x3f6d3ec4,0x3f353f35,0xbec43f6d,0xbf35bf35,0x3ec4bf6d,
0x8000bf80,0x3f800000,0x8000bf80,0x3f800000,0xbf800000,0xbf80,0xbf800000,0xbf80,0x3f353f35,0xbf6d3ec4,0xbf35bf35,0x3f6dbec4,0x3f35bf35,0x3ec43f6d,0xbf353f35,0xbec4bf6d,
0xbf800000,0x3f35bf35,0xbf800000,0x3f35bf35,0x3f80,0xbf35bf35,0x3f80,0xbf35bf35,0x3f35bf35,0xbec43f6d,0xbf353f35,0x3ec4bf6d,0xbf35bf35,0x3f6d3ec4,0x3f353f35,0xbf6dbec4,
0x80003f80,0x8000bf80,0x80003f80,0x8000bf80,0x3f800000,0xbf800000,0x3f800000,0xbf800000,0x80003f80,0x3ec4bf6d,0x8000bf80,0xbec43f6d,0x3f800000,0xbf6dbec4,0xbf800000,0x3f6d3ec4,
0x80003f80,0x3f35bf35,0x80003f80,0x3f35bf35,0x3f800000,0xbf35bf35,0x3f800000,0xbf35bf35,0x80003f80,0x3f6dbec4,0x8000bf80,0xbf6d3ec4,0x3f800000,0xbec4bf6d,0xbf800000,0x3ec43f6d,
0x80003f80,0x3f800000,0x80003f80,0x3f800000,0x3f800000,0xbf80,0x3f800000,0xbf80,0x80003f80,0x3f6d3ec4,0x8000bf80,0xbf6dbec4,0x3f800000,0x3ec4bf6d,0xbf800000,0xbec43f6d,
0x80003f80,0x3f353f35,0x80003f80,0x3f353f35,0x3f800000,0x3f35bf35,0x3f800000,0x3f35bf35,0x80003f80,0x3ec43f6d,0x8000bf80,0xbec4bf6d,0x3f800000,0x3f6dbec4,0xbf800000,0xbf6d3ec4,
0x80003f80,0x8000bf80,0x80003f80,0x8000bf80,0x3f800000,0xbf800000,0x3f800000,0xbf800000,0xbf353f35,0x3f6dbec4,0x3f35bf35,0xbf6d3ec4,0x3f353f35,0xbec4bf6d,0xbf35bf35,0x3ec43f6d,
0xbf800000,0x3f353f35,0xbf800000,0x3f353f35,0x3f80,0x3f35bf35,0x3f80,0x3f35bf35,0xbf35bf35,0xbec43f6d,0x3f353f35,0x3ec4bf6d,0xbf353f35,0x3f6d3ec4,0x3f35bf35,0xbf6dbec4,
0x8000bf80,0xbf800000,0x8000bf80,0xbf800000,0xbf800000,0x3f80,0xbf800000,0x3f80,0x3f35bf35,0xbec4bf6d,0xbf353f35,0x3ec43f6d,0xbf35bf35,0xbf6d3ec4,0x3f353f35,0x3f6dbec4,
0x3f800000,0x3f35bf35,0x3f800000,0x3f35bf35,0xbf80,0xbf35bf35,0xbf80,0xbf35bf35,0x3f353f35,0x3f6d3ec4,0xbf35bf35,0xbf6dbec4,0x3f35bf35,0x3ec4bf6d,0xbf353f35,0xbec43f6d,
0x80003f80,0x8000bf80,0x80003f80,0x8000bf80,0x3f800000,0xbf800000,0x3f800000,0xbf800000,0xbf800000,0x3f6d3ec4,0x3f800000,0xbf6dbec4,0x3f80,0x3ec4bf6d,0xbf80,0xbec43f6d,
0x8000bf80,0xbf353f35,0x8000bf80,0xbf353f35,0xbf800000,0x3f353f35,0xbf800000,0x3f353f35,0x3f800000,0xbec4bf6d,0xbf800000,0x3ec43f6d,0xbf80,0xbf6d3ec4,0x3f80,0x3f6dbec4,
0x80003f80,0x3f800000,0x80003f80,0x3f800000,0x3f800000,0xbf80,0x3f800000,0xbf80,0xbf800000,0xbec43f6d,0x3f800000,0x3ec4bf6d,0x3f80,0x3f6d3ec4,0xbf80,0xbf6dbec4,
0x8000bf80,0xbf35bf35,0x8000bf80,0xbf35bf35,0xbf800000,0xbf353f35,0xbf800000,0xbf353f35,0x3f800000,0x3f6dbec4,0xbf800000,0xbf6d3ec4,0xbf80,0xbec4bf6d,0x3f80,0x3ec43f6d,
0x80003f80,0x8000bf80,0x80003f80,0x8000bf80,0x3f800000,0xbf800000,0x3f800000,0xbf800000,0xbf35bf35,0x3ec43f6d,0x3f353f35,0xbec4bf6d,0xbf353f35,0x3f6dbec4,0x3f35bf35,0xbf6d3ec4,
0x3f800000,0xbf35bf35,0x3f800000,0xbf35bf35,0xbf80,0xbf353f35,0xbf80,0xbf353f35,0xbf353f35,0x3f6d3ec4,0x3f35bf35,0xbf6dbec4,0x3f353f35,0x3ec4bf6d,0xbf35bf35,0xbec43f6d,
0x8000bf80,0xbf800000,0x8000bf80,0xbf800000,0xbf800000,0x3f80,0xbf800000,0x3f80,0x3f353f35,0x3f6dbec4,0xbf35bf35,0xbf6d3ec4,0x3f35bf35,0xbec4bf6d,0xbf353f35,0x3ec43f6d,
0xbf800000,0xbf353f35,0xbf800000,0xbf353f35,0x3f80,0x3f353f35,0x3f80,0x3f353f35,0x3f35bf35,0x3ec4bf6d,0xbf353f35,0xbec43f6d,0xbf35bf35,0xbf6dbec4,0x3f353f35,0x3f6d3ec4,
0x80003680,0x80003680,0x80003680,0x80003680,0x36800000,0x36800000,0x36800000,0x36800000,0x80003680,0xb5c4366d,0xb6353635,0xb66d35c4,0x36800000,0x366d35c4,0x36353635,0x35c4366d,
0x80003680,0xb6353635,0xb6800000,0xb635b635,0x36800000,0x36353635,0x3680,0xb6353635,0x80003680,0xb66d35c4,0xb635b635,0x35c4b66d,0x36800000,0x35c4366d,0xb6353635,0xb66db5c4,
0x80003680,0xb6800000,0x8000b680,0x36800000,0x36800000,0x3680,0xb6800000,0xb680,0x80003680,0xb66db5c4,0x3635b635,0x35c4366d,0x36800000,0xb5c4366d,0xb635b635,0x366db5c4,
0x80003680,0xb635b635,0x36800000,0xb6353635,0x36800000,0xb6353635,0xb680,0x36353635,0x80003680,0xb5c4b66d,0x36353635,0xb66db5c4,0x36800000,0xb66d35c4,0x3635b635,0xb5c4366d,
0x80003680,0x80003680,0x80003680,0x80003680,0x36800000,0x36800000,0x36800000,0x36800000,0xb6800000,0xb66db5c4,0xb635b635,0xb5c4b66d,0x3680,0xb5c4366d,0xb6353635,0xb66d35c4,
0x8000b680,0x3635b635,0x36800000,0x36353635,0xb6800000,0xb635b635,0xb680,0x3635b635,0x36800000,0x35c4366d,0xb6353635,0xb66db5c4,0xb680,0x366db5c4,0x36353635,0xb5c4366d,
0x80003680,0xb6800000,0x8000b680,0x36800000,0x36800000,0x3680,0xb6800000,0xb680,0xb6800000,0x35c4b66d,0x36353635,0xb66d35c4,0x3680,0xb66db5c4,0x3635b635,0x35c4366d,
0x8000b680,0x36353635,0xb6800000,0x3635b635,0xb6800000,0x3635b635,0x3680,0xb635b635,0x36800000,0xb66d35c4,0x3635b635,0xb5c4366d,0xb680,0x35c4366d,0xb635b635,0x366d35c4,
0x80003680,0x80003680,0x80003680,0x80003680,0x36800000,0x36800000,0x36800000,0x36800000,0x8000b680,0x35c4b66d,0x3635b635,0x366db5c4,0xb6800000,0xb66db5c4,0xb635b635,0xb5c4b66d,
0x80003680,0xb6353635,0xb6800000,0xb635b635,0x36800000,0x36353635,0x3680,0xb6353635,0x8000b680,0x366db5c4,0x36353635,0xb5c4366d,0xb6800000,0xb5c4b66d,0x3635b635,0x366d35c4,
0x80003680,0xb6800000,0x8000b680,0x36800000,0x36800000,0x3680,0xb6800000,0xb680,0x8000b680,0x366d35c4,0xb6353635,0xb5c4b66d,0xb6800000,0x35c4b66d,0x36353635,0xb66d35c4,
0x80003680,0xb635b635,0x36800000,0xb6353635,0x36800000,0xb6353635,0xb680,0x36353635,0x8000b680,0x35c4366d,0xb635b635,0x366d35c4,0xb6800000,0x366db5c4,0xb6353635,0x35c4b66d,
0x80003680,0x80003680,0x80003680,0x80003680,0x36800000,0x36800000,0x36800000,0x36800000,0x36800000,0x366d35c4,0x36353635,0x35c4366d,0xb680,0x35c4b66d,0x3635b635,0x366db5c4,
0x8000b680,0x3635b635,0x36800000,0x36353635,0xb6800000,0xb635b635,0xb680,0x3635b635,0xb6800000,0xb5c4b66d,0x3635b635,0x366d35c4,0x3680,0xb66d35c4,0xb635b635,0x35c4b66d,
0x80003680,0xb6800000,0x8000b680,0x36800000,0x36800000,0x3680,0xb6800000,0xb680,0x36800000,0xb5c4366d,0xb635b635,0x366db5c4,0xb680,0x366d35c4,0xb6353635,0xb5c4b66d,
0x8000b680,0x36353635,0xb6800000,0x3635b635,0xb6800000,0x3635b635,0x3680,0xb635b635,0xb6800000,0x366db5c4,0xb6353635,0x35c4b66d,0x3680,0xb5c4b66d,0x36353635,0xb66db5c4,
0x80003680,0x8000b680,0x80003680,0x8000b680,0x36800000,0xb6800000,0x36800000,0xb6800000,0x80003680,0x35c4b66d,0xb6353635,0x366db5c4,0x36800000,0xb66db5c4,0x36353635,0xb5c4b66d,
0x80003680,0x3635b635,0xb6800000,0x36353635,0x36800000,0xb635b635,0x3680,0x3635b635,0x80003680,0x366db5c4,0xb635b635,0xb5c4366d,0x36800000,0xb5c4b66d,0xb6353635,0x366d35c4,
0x80003680,0x36800000,0x8000b680,0xb6800000,0x36800000,0xb680,0xb6800000,0x3680,0x80003680,0x366d35c4,0x3635b635,0xb5c4b66d,0x36800000,0x35c4b66d,0xb635b635,0xb66d35c4,
0x80003680,0x36353635,0x36800000,0x3635b635,0x36800000,0x3635b635,0xb680,0xb635b635,0x80003680,0x35c4366d,0x36353635,0x366d35c4,0x36800000,0x366db5c4,0x3635b635,0x35c4b66d,
0x80003680,0x8000b680,0x80003680,0x8000b680,0x36800000,0xb6800000,0x36800000,0xb6800000,0xb6800000,0x366d35c4,0xb635b635,0x35c4366d,0x3680,0x35c4b66d,0xb6353635,0x366db5c4,
0x8000b680,0xb6353635,0x36800000,0xb635b635,0xb6800000,0x36353635,0xb680,0xb6353635,0x36800000,0xb5c4b66d,0xb6353635,0x366d35c4,0xb680,0xb66d35c4,0x36353635,0x35c4b66d,
0x80003680,0x36800000,0x8000b680,0xb6800000,0x36800000,0xb680,0xb6800000,0x3680,0xb6800000,0xb5c4366d,0x36353635,0x366db5c4,0x3680,0x366d35c4,0x3635b635,0xb5c4b66d,
0x8000b680,0xb635b635,0xb6800000,0xb6353635,0xb6800000,0xb6353635,0x3680,0x36353635,0x36800000,0x366db5c4,0x3635b635,0x35c4b66d,0xb680,0xb5c4b66d,0xb635b635,0xb66db5c4,
0x80003680,0x8000b680,0x80003680,0x8000b680,0x36800000,0xb6800000,0x36800000,0xb6800000,0x8000b680,0xb5c4366d,0x3635b635,0xb66d35c4,0xb6800000,0x366d35c4,0xb635b635,0x35c4366d,
0x80003680,0x3635b635,0xb6800000,0x36353635,0x36800000,0xb635b635,0x3680,0x3635b635,0x8000b680,0xb66d35c4,0x36353635,0x35c4b66d,0xb6800000,0x35c4366d,0x3635b635,0xb66db5c4,
0x80003680,0x36800000,0x8000b680,0xb6800000,0x36800000,0xb680,0xb6800000,0x3680,0x8000b680,0xb66db5c4,0xb6353635,0x35c4366d,0xb6800000,0xb5c4366d,0x36353635,0x366db5c4,
0x80003680,0x36353635,0x36800000,0x3635b635,0x36800000,0x3635b635,0xb680,0xb635b635,0x8000b680,0xb5c4b66d,0xb635b635,0xb66db5c4,0xb6800000,0xb66d35c4,0xb6353635,0xb5c4366d,
0x80003680,0x8000b680,0x80003680,0x8000b680,0x36800000,0xb6800000,0x36800000,0xb6800000,0x36800000,0xb66db5c4,0x36353635,0xb5c4b66d,0xb680,0xb5c4366d,0x3635b635,0xb66d35c4,
0x8000b680,0xb6353635,0x36800000,0xb635b635,0xb6800000,0x36353635,0xb680,0xb6353635,0xb6800000,0x35c4366d,0x3635b635,0xb66db5c4,0x3680,0x366db5c4,0xb635b635,0xb5c4366d,
0x80003680,0x36800000,0x8000b680,0xb6800000,0x36800000,0xb680,0xb6800000,0x3680,0x36800000,0x35c4b66d,0xb635b635,0xb66d35c4,0xb680,0xb66db5c4,0xb6353635,0x35c4366d,
0x8000b680,0xb635b635,0xb6800000,0xb6353635,0xb6800000,0xb6353635,0x3680,0x36353635,0xb6800000,0xb66d35c4,0xb6353635,0xb5c4366d,0x3680,0x35c4366d,0x36353635,0x366d35c4,
0x80003f80,0x80003f80,0x80003f80,0x80003f80,0x80003f80,0xbe483f7b,0xbec43f6d,0xbf0e3f55,0x80003f80,0xbec43f6d,0xbf353f35,0xbf6d3ec4,0x80003f80,0xbf0e3f55,0xbf6d3ec4,0xbf7bbe48,
0x80003f80,0xbf800000,0x8000bf80,0x3f800000,0x80003f80,0xbf7bbe48,0x3ec4bf6d,0x3f553f0e,0x80003f80,0xbf6dbec4,0x3f35bf35,0x3ec43f6d,0x80003f80,0xbf55bf0e,0x3f6dbec4,0xbe483f7b,
0x80003f80,0x8000bf80,0x80003f80,0x8000bf80,0x80003f80,0x3e48bf7b,0xbec43f6d,0x3f0ebf55,0x80003f80,0x3ec4bf6d,0xbf353f35,0x3f6dbec4,0x80003f80,0x3f0ebf55,0xbf6d3ec4,0x3f7b3e48,
0x80003f80,0x3f800000,0x8000bf80,0xbf800000,0x80003f80,0x3f7b3e48,0x3ec4bf6d,0xbf55bf0e,0x80003f80,0x3f6d3ec4,0x3f35bf35,0xbec4bf6d,0x80003f80,0x3f553f0e,0x3f6dbec4,0x3e48bf7b,
0x80003f80,0x80003f80,0x80003f80,0x80003f80,0xbf353f35,0xbf553f0e,0xbf6d3ec4,0xbf7b3e48,0xbf800000,0xbf6dbec4,0xbf35bf35,0xbec4bf6d,0xbf35bf35,0xbe48bf7b,0x3ec4bf6d,0x3f55bf0e,
0x80003f80,0xbf800000,0x8000bf80,0x3f800000,0xbf353f35,0xbf0ebf55,0x3f6dbec4,0x3e483f7b,0xbf800000,0x3ec4bf6d,0x3f353f35,0xbf6d3ec4,0xbf35bf35,0x3f7bbe48,0xbec43f6d,0xbf0ebf55,
0x80003f80,0x8000bf80,0x80003f80,0x8000bf80,0xbf353f35,0x3f55bf0e,0xbf6d3ec4,0x3f7bbe48,0xbf800000,0x3f6d3ec4,0xbf35bf35,0x3ec43f6d,0xbf35bf35,0x3e483f7b,0x3ec4bf6d,0xbf553f0e,
0x80003f80,0x3f800000,0x8000bf80,0xbf800000,0xbf353f35,0x3f0e3f55,0x3f6dbec4,0xbe48bf7b,0xbf800000,0xbec43f6d,0x3f353f35,0x3f6dbec4,0xbf35bf35,0xbf7b3e48,0xbec43f6d,0x3f0e3f55,
0x80003f80,0x80003f80,0x80003f80,0x80003f80,0xbf800000,0xbf7bbe48,0xbf6dbec4,0xbf55bf0e,0x8000bf80,0x3ec4bf6d,0x3f35bf35,0x3f6dbec4,0x3f800000,0x3f553f0e,0x3ec43f6d,0xbe483f7b,
0x80003f80,0xbf800000,0x8000bf80,0x3f800000,0xbf800000,0x3e48bf7b,0x3f6d3ec4,0xbf0e3f55,0x8000bf80,0x3f6d3ec4,0xbf353f35,0xbec4bf6d,0x3f800000,0xbf0e3f55,0xbec4bf6d,0x3f7b3e48,
0x80003f80,0x8000bf80,0x80003f80,0x8000bf80,0xbf800000,0x3f7b3e48,0xbf6dbec4,0x3f553f0e,0x8000bf80,0xbec43f6d,0x3f35bf35,0xbf6d3ec4,0x3f800000,0xbf55bf0e,0x3ec43f6d,0x3e48bf7b,
0x80003f80,0x3f800000,0x8000bf80,0xbf800000,0xbf800000,0xbe483f7b,0x3f6d3ec4,0x3f0ebf55,0x8000bf80,0xbf6dbec4,0xbf353f35,0x3ec43f6d,0x3f800000,0x3f0ebf55,0xbec4bf6d,0xbf7bbe48,
0x80003f80,0x80003f80,0x80003f80,0x80003f80,0xbf35bf35,0xbf0ebf55,0xbec4bf6d,0xbe48bf7b,0x3f800000,0x3f6d3ec4,0x3f353f35,0x3ec43f6d,0xbf353f35,0xbf7b3e48,0xbf6dbec4,0xbf0ebf55,
0x80003f80,0xbf800000,0x8000bf80,0x3f800000,0xbf35bf35,0x3f55bf0e,0x3ec43f6d,0xbf7b3e48,0x3f800000,0xbec43f6d,0xbf35bf35,0x3f6dbec4,0xbf353f35,0xbe48bf7b,0x3f6d3ec4,0xbf553f0e,
0x80003f80,0x8000bf80,0x80003f80,0x8000bf80,0xbf35bf35,0x3f0e3f55,0xbec4bf6d,0x3e483f7b,0x3f800000,0xbf6dbec4,0x3f353f35,0xbec4bf6d,0xbf353f35,0x3f7bbe48,0xbf6dbec4,0x3f0e3f55,
0x80003f80,0x3f800000,0x8000bf80,0xbf800000,0xbf35bf35,0xbf553f0e,0x3ec43f6d,0x3f7bbe48,0x3f800000,0x3ec4bf6d,0xbf35bf35,0xbf6d3ec4,0xbf353f35,0x3e483f7b,0x3f6d3ec4,0x3f55bf0e,
0x80003f80,0xbf353f35,0xbf800000,0xbf35bf35,0x80003f80,0xbf553f0e,0xbf6dbec4,0xbe48bf7b,0x80003f80,0xbf6d3ec4,0xbf35bf35,0x3ec4bf6d,0x80003f80,0xbf7b3e48,0xbec4bf6d,0x3f55bf0e,
0x80003f80,0xbf35bf35,0x3f800000,0xbf353f35,0x80003f80,0xbf0ebf55,0x3f6d3ec4,0xbf7b3e48,0x80003f80,0xbec4bf6d,0x3f353f35,0xbf6dbec4,0x80003f80,0xbe48bf7b,0x3ec43f6d,0xbf0ebf55,
0x80003f80,0x3f35bf35,0xbf800000,0x3f353f35,0x80003f80,0x3f55bf0e,0xbf6dbec4,0x3e483f7b,0x80003f80,0x3f6dbec4,0xbf35bf35,0xbec43f6d,0x80003f80,0x3f7bbe48,0xbec4bf6d,0xbf553f0e,
0x80003f80,0x3f353f35,0x3f800000,0x3f35bf35,0x80003f80,0x3f0e3f55,0x3f6d3ec4,0x3f7bbe48,0x80003f80,0x3ec43f6d,0x3f353f35,0x3f6d3ec4,0x80003f80,0x3e483f7b,0x3ec43f6d,0x3f0e3f55,
0x8000bf80,0x3f35bf35,0x3f800000,0x3f353f35,0x3f35bf35,0x3f7b3e48,0x3ec43f6d,0xbf0e3f55,0x3f800000,0x3ec43f6d,0xbf353f35,0xbf6dbec4,0x3f353f35,0xbf0e3f55,0xbf6dbec4,0x3e48bf7b,
0x8000bf80,0x3f353f35,0xbf800000,0x3f35bf35,0x3f35bf35,0xbe483f7b,0xbec4bf6d,0x3f553f0e,0x3f800000,0xbf6d3ec4,0x3f35bf35,0xbec43f6d,0x3f353f35,0xbf55bf0e,0x3f6d3ec4,0xbf7bbe48,
0x8000bf80,0xbf353f35,0x3f800000,0xbf35bf35,0x3f35bf35,0xbf7bbe48,0x3ec43f6d,0x3f0ebf55,0x3f800000,0xbec4bf6d,0xbf353f35,0x3f6d3ec4,0x3f353f35,0x3f0ebf55,0xbf6dbec4,0xbe483f7b,
0x8000bf80,0xbf35bf35,0xbf800000,0xbf353f35,0x3f35bf35,0x3e48bf7b,0xbec4bf6d,0xbf55bf0e,0x3f800000,0x3f6dbec4,0x3f35bf35,0x3ec4bf6d,0x3f353f35,0x3f553f0e,0x3f6d3ec4,0x3f7b3e48,
0x80003f80,0xbf353f35,0xbf800000,0xbf35bf35,0xbf800000,0xbf0ebf55,0x3ec4bf6d,0x3f7bbe48,0x8000bf80,0x3f6dbec4,0x3f353f35,0xbec43f6d,0x3f800000,0x3e483f7b,0xbf6d3ec4,0xbf0ebf55,
0x80003f80,0xbf35bf35,0x3f800000,0xbf353f35,0xbf800000,0x3f55bf0e,0xbec43f6d,0xbe48bf7b,0x8000bf80,0x3ec43f6d,0xbf35bf35,0x3f6d3ec4,0x3f800000,0xbf7b3e48,0x3f6dbec4,0xbf553f0e,
0x80003f80,0x3f35bf35,0xbf800000,0x3f353f35,0xbf800000,0x3f0e3f55,0x3ec4bf6d,0xbf7b3e48,0x8000bf80,0xbf6d3ec4,0x3f353f35,0x3ec4bf6d,0x3f800000,0xbe48bf7b,0xbf6d3ec4,0x3f0e3f55,
0x80003f80,0x3f353f35,0x3f800000,0x3f35bf35,0xbf800000,0xbf553f0e,0xbec43f6d,0x3e483f7b,0x8000bf80,0xbec4bf6d,0xbf35bf35,0xbf6dbec4,0x3f800000,0x3f7bbe48,0x3f6dbec4,0x3f55bf0e,
0x8000bf80,0x3f35bf35,0x3f800000,0x3f353f35,0x3f353f35,0xbe483f7b,0xbf6d3ec4,0xbf55bf0e,0xbf800000,0xbec4bf6d,0x3f35bf35,0x3f6d3ec4,0x3f35bf35,0x3f553f0e,0xbec43f6d,0xbf7bbe48,
0x8000bf80,0x3f353f35,0xbf800000,0x3f35bf35,0x3f353f35,0xbf7bbe48,0x3f6dbec4,0xbf0e3f55,0xbf800000,0x3f6dbec4,0xbf353f35,0x3ec4bf6d,0x3f35bf35,0xbf0e3f55,0x3ec4bf6d,0xbe483f7b,
0x8000bf80,0xbf353f35,0x3f800000,0xbf35bf35,0x3f353f35,0x3e48bf7b,0xbf6d3ec4,0x3f553f0e,0xbf800000,0x3ec43f6d,0x3f35bf35,0xbf6dbec4,0x3f35bf35,0xbf55bf0e,0xbec43f6d,0x3f7b3e48,
0x8000bf80,0xbf35bf35,0xbf800000,0xbf353f35,0x3f353f35,0x3f7b3e48,0x3f6dbec4,0x3f0ebf55,0xbf800000,0xbf6d3ec4,0xbf353f35,0xbec43f6d,0x3f35bf35,0x3f0ebf55,0x3ec4bf6d,0x3e48bf7b,
0x80003f80,0x80003f80,0x80003f80,0x80003f80,0x8000bf80,0x3e48bf7b,0x3ec4bf6d,0x3f0ebf55,0x80003f80,0xbec43f6d,0xbf353f35,0xbf6d3ec4,0x8000bf80,0x3f0ebf55,0x3f6dbec4,0x3f7b3e48,
0x80003f80,0xbf800000,0x8000bf80,0x3f800000,0x8000bf80,0x3f7b3e48,0xbec43f6d,0xbf55bf0e,0x80003f80,0xbf6dbec4,0x3f35bf35,0x3ec43f6d,0x8000bf80,0x3f553f0e,0xbf6d3ec4,0x3e48bf7b,
0x80003f80,0x8000bf80,0x80003f80,0x8000bf80,0x8000bf80,0xbe483f7b,0x3ec4bf6d,0xbf0e3f55,0x80003f80,0x3ec4bf6d,0xbf353f35,0x3f6dbec4,0x8000bf80,0xbf0e3f55,0x3f6dbec4,0xbf7bbe48,
0x80003f80,0x3f800000,0x8000bf80,0xbf800000,0x8000bf80,0xbf7bbe48,0xbec43f6d,0x3f553f0e,0x80003f80,0x3f6d3ec4,0x3f35bf35,0xbec4bf6d,0x8000bf80,0xbf55bf0e,0xbf6d3ec4,0xbe483f7b,
0x80003f80,0x80003f80,0x80003f80,0x80003f80,0x3f35bf35,0x3f55bf0e,0x3f6dbec4,0x3f7bbe48,0xbf800000,0xbf6dbec4,0xbf35bf35,0xbec4bf6d,0x3f353f35,0x3e483f7b,0xbec43f6d,0xbf553f0e,
0x80003f80,0xbf800000,0x8000bf80,0x3f800000,0x3f35bf35,0x3f0e3f55,0xbf6d3ec4,0xbe48bf7b,0xbf800000,0x3ec4bf6d,0x3f353f35,0xbf6d3ec4,0x3f353f35,0xbf7b3e48,0x3ec4bf6d,0x3f0e3f55,
0x80003f80,0x8000bf80,0x80003f80,0x8000bf80,0x3f35bf35,0xbf553f0e,0x3f6dbec4,0xbf7b3e48,0xbf800000,0x3f6d3ec4,0xbf35bf35,0x3ec43f6d,0x3f353f35,0xbe48bf7b,0xbec43f6d,0x3f55bf0e,
0x80003f80,0x3f800000,0x8000bf80,0xbf800000,0x3f35bf35,0xbf0ebf55,0xbf6d3ec4,0x3e483f7b,0xbf800000,0xbec43f6d,0x3f353f35,0x3f6dbec4,0x3f353f35,0x3f7bbe48,0x3ec4bf6d,0xbf0ebf55,
0x80003f80,0x80003f80,0x80003f80,0x80003f80,0x3f800000,0x3f7b3e48,0x3f6d3ec4,0x3f553f0e,0x8000bf80,0x3ec4bf6d,0x3f35bf35,0x3f6dbec4,0xbf800000,0xbf55bf0e,0xbec4bf6d,0x3e48bf7b,
0x80003f80,0xbf800000,0x8000bf80,0x3f800000,0x3f800000,0xbe483f7b,0xbf6dbec4,0x3f0ebf55,0x8000bf80,0x3f6d3ec4,0xbf353f35,0xbec4bf6d,0xbf800000,0x3f0ebf55,0x3ec43f6d,0xbf7bbe48,
0x80003f80,0x8000bf80,0x80003f80,0x8000bf80,0x3f800000,0xbf7bbe48,0x3f6d3ec4,0xbf55bf0e,0x8000bf80,0xbec43f6d,0x3f35bf35,0xbf6d3ec4,0xbf800000,0x3f553f0e,0xbec4bf6d,0xbe483f7b,
0x80003f80,0x3f800000,0x8000bf80,0xbf800000,0x3f800000,0x3e48bf7b,0xbf6dbec4,0xbf0e3f55,0x8000bf80,0xbf6dbec4,0xbf353f35,0x3ec43f6d,0xbf800000,0xbf0e3f55,0x3ec43f6d,0x3f7b3e48,
0x80003f80,0x80003f80,0x80003f80,0x80003f80,0x3f353f35,0x3f0e3f55,0x3ec43f6d,0x3e483f7b,0x3f800000,0x3f6d3ec4,0x3f353f35,0x3ec43f6d,0x3f35bf35,0x3f7bbe48,0x3f6d3ec4,0x3f0e3f55,
0x80003f80,0xbf800000,0x8000bf80,0x3f800000,0x3f353f35,0xbf553f0e,0xbec4bf6d,0x3f7bbe48,0x3f800000,0xbec43f6d,0xbf35bf35,0x3f6dbec4,0x3f35bf35,0x3e483f7b,0xbf6dbec4,0x3f55bf0e,
0x80003f80,0x8000bf80,0x80003f80,0x8000bf80,0x3f353f35,0xbf0ebf55,0x3ec43f6d,0xbe48bf7b,0x3f800000,0xbf6dbec4,0x3f353f35,0xbec4bf6d,0x3f35bf35,0xbf7b3e48,0x3f6d3ec4,0xbf0ebf55,
0x80003f80,0x3f800000,0x8000bf80,0xbf800000,0x3f353f35,0x3f55bf0e,0xbec4bf6d,0xbf7b3e48,0x3f800000,0x3ec4bf6d,0xbf35bf35,0xbf6d3ec4,0x3f35bf35,0xbe48bf7b,0xbf6dbec4,0xbf553f0e,
0x80003f80,0xbf353f35,0xbf800000,0xbf35bf35,0x8000bf80,0x3f55bf0e,0x3f6d3ec4,0x3e483f7b,0x80003f80,0xbf6d3ec4,0xbf35bf35,0x3ec4bf6d,0x8000bf80,0x3f7bbe48,0x3ec43f6d,0xbf553f0e,
0x80003f80,0xbf35bf35,0x3f800000,0xbf353f35,0x8000bf80,0x3f0e3f55,0xbf6dbec4,0x3f7bbe48,0x80003f80,0xbec4bf6d,0x3f353f35,0xbf6dbec4,0x8000bf80,0x3e483f7b,0xbec4bf6d,0x3f0e3f55,
0x80003f80,0x3f35bf35,0xbf800000,0x3f353f35,0x8000bf80,0xbf553f0e,0x3f6d3ec4,0xbe48bf7b,0x80003f80,0x3f6dbec4,0xbf35bf35,0xbec43f6d,0x8000bf80,0xbf7b3e48,0x3ec43f6d,0x3f55bf0e,
0x80003f80,0x3f353f35,0x3f800000,0x3f35bf35,0x8000bf80,0xbf0ebf55,0xbf6dbec4,0xbf7b3e48,0x80003f80,0x3ec43f6d,0x3f353f35,0x3f6d3ec4,0x8000bf80,0xbe48bf7b,0xbec4bf6d,0xbf0ebf55,
0x8000bf80,0x3f35bf35,0x3f800000,0x3f353f35,0xbf353f35,0xbf7bbe48,0xbec4bf6d,0x3f0ebf55,0x3f800000,0x3ec43f6d,0xbf353f35,0xbf6dbec4,0xbf35bf35,0x3f0ebf55,0x3f6d3ec4,0xbe483f7b,
0x8000bf80,0x3f353f35,0xbf800000,0x3f35bf35,0xbf353f35,0x3e48bf7b,0x3ec43f6d,0xbf55bf0e,0x3f800000,0xbf6d3ec4,0x3f35bf35,0xbec43f6d,0xbf35bf35,0x3f553f0e,0xbf6dbec4,0x3f7b3e48,
0x8000bf80,0xbf353f35,0x3f800000,0xbf35bf35,0xbf353f35,0x3f7b3e48,0xbec4bf6d,0xbf0e3f55,0x3f800000,0xbec4bf6d,0xbf353f35,0x3f6d3ec4,0xbf35bf35,0xbf0e3f55,0x3f6d3ec4,0x3e48bf7b,
0x8000bf80,0xbf35bf35,0xbf800000,0xbf353f35,0xbf353f35,0xbe483f7b,0x3ec43f6d,0x3f553f0e,0x3f800000,0x3f6dbec4,0x3f35bf35,0x3ec4bf6d,0xbf35bf35,0xbf55bf0e,0xbf6dbec4,0xbf7bbe48,
0x80003f80,0xbf353f35,0xbf800000,0xbf35bf35,0x3f800000,0x3f0e3f55,0xbec43f6d,0xbf7b3e48,0x8000bf80,0x3f6dbec4,0x3f353f35,0xbec43f6d,0xbf800000,0xbe48bf7b,0x3f6dbec4,0x3f0e3f55,
0x80003f80,0xbf35bf35,0x3f800000,0xbf353f35,0x3f800000,0xbf553f0e,0x3ec4bf6d,0x3e483f7b,0x8000bf80,0x3ec43f6d,0xbf35bf35,0x3f6d3ec4,0xbf800000,0x3f7bbe48,0xbf6d3ec4,0x3f55bf0e,
0x80003f80,0x3f35bf35,0xbf800000,0x3f353f35,0x3f800000,0xbf0ebf55,0xbec43f6d,0x3f7bbe48,0x8000bf80,0xbf6d3ec4,0x3f353f35,0x3ec4bf6d,0xbf800000,0x3e483f7b,0x3f6dbec4,0xbf0ebf55,
0x80003f80,0x3f353f35,0x3f800000,0x3f35bf35,0x3f800000,0x3f55bf0e,0x3ec4bf6d,0xbe48bf7b,0x8000bf80,0xbec4bf6d,0xbf35bf35,0xbf6dbec4,0xbf800000,0xbf7b3e48,0xbf6d3ec4,0xbf553f0e,
0x8000bf80,0x3f35bf35,0x3f800000,0x3f353f35,0xbf35bf35,0x3e48bf7b,0x3f6dbec4,0x3f553f0e,0xbf800000,0xbec4bf6d,0x3f35bf35,0x3f6d3ec4,0xbf353f35,0xbf55bf0e,0x3ec4bf6d,0x3f7b3e48,
0x8000bf80,0x3f353f35,0xbf800000,0x3f35bf35,0xbf35bf35,0x3f7b3e48,0xbf6d3ec4,0x3f0ebf55,0xbf800000,0x3f6dbec4,0xbf353f35,0x3ec4bf6d,0xbf353f35,0x3f0ebf55,0xbec43f6d,0x3e48bf7b,
0x8000bf80,0xbf353f35,0x3f800000,0xbf35bf35,0xbf35bf35,0xbe483f7b,0x3f6dbec4,0xbf55bf0e,0xbf800000,0x3ec43f6d,0x3f35bf35,0xbf6dbec4,0xbf353f35,0x3f553f0e,0x3ec4bf6d,0xbf7bbe48,
0x8000bf80,0xbf35bf35,0xbf800000,0xbf353f35,0xbf35bf35,0xbf7bbe48,0xbf6d3ec4,0xbf0e3f55,0xbf800000,0xbf6d3ec4,0xbf353f35,0xbec43f6d,0xbf353f35,0xbf0e3f55,0xbec43f6d,0xbe483f7b,
0x3f800000,0x3f800000,0x3f800000,0x3f800000,0x3f800000,0x3f7b3e48,0x3f6d3ec4,0x3f553f0e,0x3f800000,0x3f6d3ec4,0x3f353f35,0x3ec43f6d,0x3f800000,0x3f553f0e,0x3ec43f6d,0xbe483f7b,
0x3f800000,0x3f80,0xbf800000,0xbf80,0x3f800000,0xbe483f7b,0xbf6dbec4,0x3f0ebf55,0x3f800000,0xbec43f6d,0xbf35bf35,0x3f6dbec4,0x3f800000,0xbf0e3f55,0xbec4bf6d,0x3f7b3e48,
0x3f800000,0xbf800000,0x3f800000,0xbf800000,0x3f800000,0xbf7bbe48,0x3f6d3ec4,0xbf55bf0e,0x3f800000,0xbf6dbec4,0x3f353f35,0xbec4bf6d,0x3f800000,0xbf55bf0e,0x3ec43f6d,0x3e48bf7b,
0x3f800000,0xbf80,0xbf800000,0x3f80,0x3f800000,0x3e48bf7b,0xbf6dbec4,0xbf0e3f55,0x3f800000,0x3ec4bf6d,0xbf35bf35,0xbf6d3ec4,0x3f800000,0x3f0ebf55,0xbec4bf6d,0xbf7bbe48,
0x3f800000,0x3f800000,0x3f800000,0x3f800000,0x3f353f35,0x3f0e3f55,0x3ec43f6d,0x3e483f7b,0x3f80,0xbec43f6d,0xbf353f35,0xbf6d3ec4,0xbf353f35,0xbf7b3e48,0xbf6dbec4,0xbf0ebf55,
0x3f800000,0x3f80,0xbf800000,0xbf80,0x3f353f35,0xbf553f0e,0xbec4bf6d,0x3f7bbe48,0x3f80,0xbf6dbec4,0x3f35bf35,0x3ec43f6d,0xbf353f35,0xbe48bf7b,0x3f6d3ec4,0xbf553f0e,
0x3f800000,0xbf800000,0x3f800000,0xbf800000,0x3f353f35,0xbf0ebf55,0x3ec43f6d,0xbe48bf7b,0x3f80,0x3ec4bf6d,0xbf353f35,0x3f6dbec4,0xbf353f35,0x3f7bbe48,0xbf6dbec4,0x3f0e3f55,
0x3f800000,0xbf80,0xbf800000,0x3f80,0x3f353f35,0x3f55bf0e,0xbec4bf6d,0xbf7b3e48,0x3f80,0x3f6d3ec4,0x3f35bf35,0xbec4bf6d,0xbf353f35,0x3e483f7b,0x3f6d3ec4,0x3f55bf0e,
0x3f800000,0x3f800000,0x3f800000,0x3f800000,0x3f80,0xbe483f7b,0xbec43f6d,0xbf0e3f55,0xbf800000,0xbf6dbec4,0xbf35bf35,0xbec4bf6d,0xbf80,0x3f0ebf55,0x3f6dbec4,0x3f7b3e48,
0x3f800000,0x3f80,0xbf800000,0xbf80,0x3f80,0xbf7bbe48,0x3ec4bf6d,0x3f553f0e,0xbf800000,0x3ec4bf6d,0x3f353f35,0xbf6d3ec4,0xbf80,0x3f553f0e,0xbf6d3ec4,0x3e48bf7b,
0x3f800000,0xbf800000,0x3f800000,0xbf800000,0x3f80,0x3e48bf7b,0xbec43f6d,0x3f0ebf55,0xbf800000,0x3f6d3ec4,0xbf35bf35,0x3ec43f6d,0xbf80,0xbf0e3f55,0x3f6dbec4,0xbf7bbe48,
0x3f800000,0xbf80,0xbf800000,0x3f80,0x3f80,0x3f7b3e48,0x3ec4bf6d,0xbf55bf0e,0xbf800000,0xbec43f6d,0x3f353f35,0x3f6dbec4,0xbf80,0xbf55bf0e,0xbf6d3ec4,0xbe483f7b,
0x3f800000,0x3f800000,0x3f800000,0x3f800000,0xbf353f35,0xbf553f0e,0xbf6d3ec4,0xbf7b3e48,0xbf80,0x3ec4bf6d,0x3f35bf35,0x3f6dbec4,0x3f353f35,0x3e483f7b,0xbec43f6d,0xbf553f0e,
0x3f800000,0x3f80,0xbf800000,0xbf80,0xbf353f35,0xbf0ebf55,0x3f6dbec4,0x3e483f7b,0xbf80,0x3f6d3ec4,0xbf353f35,0xbec4bf6d,0x3f353f35,0xbf7b3e48,0x3ec4bf6d,0x3f0e3f55,
0x3f800000,0xbf800000,0x3f800000,0xbf800000,0xbf353f35,0x3f55bf0e,0xbf6d3ec4,0x3f7bbe48,0xbf80,0xbec43f6d,0x3f35bf35,0xbf6d3ec4,0x3f353f35,0xbe48bf7b,0xbec43f6d,0x3f55bf0e,
0x3f800000,0xbf80,0xbf800000,0x3f80,0xbf353f35,0x3f0e3f55,0x3f6dbec4,0xbe48bf7b,0xbf80,0xbf6dbec4,0xbf353f35,0x3ec43f6d,0x3f353f35,0x3f7bbe48,0x3ec4bf6d,0xbf0ebf55,
0x3f800000,0x3f353f35,0x3f80,0xbf353f35,0x3f800000,0x3f0e3f55,0xbec43f6d,0xbf7b3e48,0x3f800000,0x3ec43f6d,0xbf353f35,0xbf6dbec4,0x3f800000,0x3e483f7b,0xbf6d3ec4,0xbf0ebf55,
0x3f800000,0xbf353f35,0xbf80,0x3f353f35,0x3f800000,0xbf553f0e,0x3ec4bf6d,0x3e483f7b,0x3f800000,0xbf6d3ec4,0x3f35bf35,0xbec43f6d,0x3f800000,0xbf7b3e48,0x3f6dbec4,0xbf553f0e,
0x3f800000,0xbf35bf35,0x3f80,0x3f35bf35,0x3f800000,0xbf0ebf55,0xbec43f6d,0x3f7bbe48,0x3f800000,0xbec4bf6d,0xbf353f35,0x3f6d3ec4,0x3f800000,0xbe48bf7b,0xbf6d3ec4,0x3f0e3f55,
0x3f800000,0x3f35bf35,0xbf80,0xbf35bf35,0x3f800000,0x3f55bf0e,0x3ec4bf6d,0xbe48bf7b,0x3f800000,0x3f6dbec4,0x3f35bf35,0x3ec4bf6d,0x3f800000,0x3f7bbe48,0x3f6dbec4,0x3f55bf0e,
0xbf800000,0xbf35bf35,0xbf80,0x3f35bf35,0xbf35bf35,0x3e48bf7b,0x3f6dbec4,0x3f553f0e,0xbf80,0x3f6dbec4,0x3f353f35,0xbec43f6d,0x3f35bf35,0x3f553f0e,0xbec43f6d,0xbf7bbe48,
0xbf800000,0x3f35bf35,0x3f80,0xbf35bf35,0xbf35bf35,0x3f7b3e48,0xbf6d3ec4,0x3f0ebf55,0xbf80,0x3ec43f6d,0xbf35bf35,0x3f6d3ec4,0x3f35bf35,0xbf0e3f55,0x3ec4bf6d,0xbe483f7b,
0xbf800000,0x3f353f35,0xbf80,0xbf353f35,0xbf35bf35,0xbe483f7b,0x3f6dbec4,0xbf55bf0e,0xbf80,0xbf6d3ec4,0x3f353f35,0x3ec4bf6d,0x3f35bf35,0xbf55bf0e,0xbec43f6d,0x3f7b3e48,
0xbf800000,0xbf353f35,0x3f80,0x3f353f35,0xbf35bf35,0xbf7bbe48,0xbf6d3ec4,0xbf0e3f55,0xbf80,0xbec4bf6d,0xbf35bf35,0xbf6dbec4,0x3f35bf35,0x3f0ebf55,0x3ec4bf6d,0x3e48bf7b,
0x3f800000,0x3f353f35,0x3f80,0xbf353f35,0x3f80,0xbf553f0e,0xbf6dbec4,0xbe48bf7b,0xbf800000,0xbec4bf6d,0x3f35bf35,0x3f6d3ec4,0xbf80,0x3f7bbe48,0x3ec43f6d,0xbf553f0e,
0x3f800000,0xbf353f35,0xbf80,0x3f353f35,0x3f80,0xbf0ebf55,0x3f6d3ec4,0xbf7b3e48,0xbf800000,0x3f6dbec4,0xbf353f35,0x3ec4bf6d,0xbf80,0x3e483f7b,0xbec4bf6d,0x3f0e3f55,
0x3f800000,0xbf35bf35,0x3f80,0x3f35bf35,0x3f80,0x3f55bf0e,0xbf6dbec4,0x3e483f7b,0xbf800000,0x3ec43f6d,0x3f35bf35,0xbf6dbec4,0xbf80,0xbf7b3e48,0x3ec43f6d,0x3f55bf0e,
0x3f800000,0x3f35bf35,0xbf80,0xbf35bf35,0x3f80,0x3f0e3f55,0x3f6d3ec4,0x3f7bbe48,0xbf800000,0xbf6d3ec4,0xbf353f35,0xbec43f6d,0xbf80,0xbe48bf7b,0xbec4bf6d,0xbf0ebf55,
0xbf800000,0xbf35bf35,0xbf80,0x3f35bf35,0x3f35bf35,0x3f7b3e48,0x3ec43f6d,0xbf0e3f55,0x3f80,0xbf6d3ec4,0xbf35bf35,0x3ec4bf6d,0xbf35bf35,0x3f0ebf55,0x3f6d3ec4,0xbe483f7b,
0xbf800000,0x3f35bf35,0x3f80,0xbf35bf35,0x3f35bf35,0xbe483f7b,0xbec4bf6d,0x3f553f0e,0x3f80,0xbec4bf6d,0x3f353f35,0xbf6dbec4,0xbf35bf35,0x3f553f0e,0xbf6dbec4,0x3f7b3e48,
0xbf800000,0x3f353f35,0xbf80,0xbf353f35,0x3f35bf35,0xbf7bbe48,0x3ec43f6d,0x3f0ebf55,0x3f80,0x3f6dbec4,0xbf35bf35,0xbec43f6d,0xbf35bf35,0xbf0e3f55,0x3f6d3ec4,0x3e48bf7b,
0xbf800000,0xbf353f35,0x3f80,0x3f353f35,0x3f35bf35,0x3e48bf7b,0xbec4bf6d,0xbf55bf0e,0x3f80,0x3ec43f6d,0x3f353f35,0x3f6d3ec4,0xbf35bf35,0xbf55bf0e,0xbf6dbec4,0xbf7bbe48,
0x3f800000,0x3f800000,0x3f800000,0x3f800000,0xbf800000,0xbf7bbe48,0xbf6dbec4,0xbf55bf0e,0x3f800000,0x3f6d3ec4,0x3f353f35,0x3ec43f6d,0xbf800000,0xbf55bf0e,0xbec4bf6d,0x3e48bf7b,
0x3f800000,0x3f80,0xbf800000,0xbf80,0xbf800000,0x3e48bf7b,0x3f6d3ec4,0xbf0e3f55,0x3f800000,0xbec43f6d,0xbf35bf35,0x3f6dbec4,0xbf800000,0x3f0ebf55,0x3ec43f6d,0xbf7bbe48,
0x3f800000,0xbf800000,0x3f800000,0xbf800000,0xbf800000,0x3f7b3e48,0xbf6dbec4,0x3f553f0e,0x3f800000,0xbf6dbec4,0x3f353f35,0xbec4bf6d,0xbf800000,0x3f553f0e,0xbec4bf6d,0xbe483f7b,
0x3f800000,0xbf80,0xbf800000,0x3f80,0xbf800000,0xbe483f7b,0x3f6d3ec4,0x3f0ebf55,0x3f800000,0x3ec4bf6d,0xbf35bf35,0xbf6d3ec4,0xbf800000,0xbf0e3f55,0x3ec43f6d,0x3f7b3e48,
0x3f800000,0x3f800000,0x3f800000,0x3f800000,0xbf35bf35,0xbf0ebf55,0xbec4bf6d,0xbe48bf7b,0x3f80,0xbec43f6d,0xbf353f35,0xbf6d3ec4,0x3f35bf35,0x3f7bbe48,0x3f6d3ec4,0x3f0e3f55,
0x3f800000,0x3f80,0xbf800000,0xbf80,0xbf35bf35,0x3f55bf0e,0x3ec43f6d,0xbf7b3e48,0x3f80,0xbf6dbec4,0x3f35bf35,0x3ec43f6d,0x3f35bf35,0x3e483f7b,0xbf6dbec4,0x3f55bf0e,
0x3f800000,0xbf800000,0x3f800000,0xbf800000,0xbf35bf35,0x3f0e3f55,0xbec4bf6d,0x3e483f7b,0x3f80,0x3ec4bf6d,0xbf353f35,0x3f6dbec4,0x3f35bf35,0xbf7b3e48,0x3f6d3ec4,0xbf0ebf55,
0x3f800000,0xbf80,0xbf800000,0x3f80,0xbf35bf35,0xbf553f0e,0x3ec43f6d,0x3f7bbe48,0x3f80,0x3f6d3ec4,0x3f35bf35,0xbec4bf6d,0x3f35bf35,0xbe48bf7b,0xbf6dbec4,0xbf553f0e,
0x3f800000,0x3f800000,0x3f800000,0x3f800000,0xbf80,0x3e48bf7b,0x3ec4bf6d,0x3f0ebf55,0xbf800000,0xbf6dbec4,0xbf35bf35,0xbec4bf6d,0x3f80,0xbf0e3f55,0xbf6d3ec4,0xbf7bbe48,
0x3f800000,0x3f80,0xbf800000,0xbf80,0xbf80,0x3f7b3e48,0xbec43f6d,0xbf55bf0e,0xbf800000,0x3ec4bf6d,0x3f353f35,0xbf6d3ec4,0x3f80,0xbf55bf0e,0x3f6dbec4,0xbe483f7b,
0x3f800000,0xbf800000,0x3f800000,0xbf800000,0xbf80,0xbe483f7b,0x3ec4bf6d,0xbf0e3f55,0xbf800000,0x3f6d3ec4,0xbf35bf35,0x3ec43f6d,0x3f80,0x3f0ebf55,0xbf6d3ec4,0x3f7b3e48,
0x3f800000,0xbf80,0xbf800000,0x3f80,0xbf80,0xbf7bbe48,0xbec43f6d,0x3f553f0e,0xbf800000,0xbec43f6d,0x3f353f35,0x3f6dbec4,0x3f80,0x3f553f0e,0x3f6dbec4,0x3e48bf7b,
0x3f800000,0x3f800000,0x3f800000,0x3f800000,0x3f35bf35,0x3f55bf0e,0x3f6dbec4,0x3f7bbe48,0xbf80,0x3ec4bf6d,0x3f35bf35,0x3f6dbec4,0xbf35bf35,0xbe48bf7b,0x3ec4bf6d,0x3f55bf0e,
0x3f800000,0x3f80,0xbf800000,0xbf80,0x3f35bf35,0x3f0e3f55,0xbf6d3ec4,0xbe48bf7b,0xbf80,0x3f6d3ec4,0xbf353f35,0xbec4bf6d,0xbf35bf35,0x3f7bbe48,0xbec43f6d,0xbf0ebf55,
0x3f800000,0xbf800000,0x3f800000,0xbf800000,0x3f35bf35,0xbf553f0e,0x3f6dbec4,0xbf7b3e48,0xbf80,0xbec43f6d,0x3f35bf35,0xbf6d3ec4,0xbf35bf35,0x3e483f7b,0x3ec4bf6d,0xbf553f0e,
0x3f800000,0xbf80,0xbf800000,0x3f80,0x3f35bf35,0xbf0ebf55,0xbf6d3ec4,0x3e483f7b,0xbf80,0xbf6dbec4,0xbf353f35,0x3ec43f6d,0xbf35bf35,0xbf7b3e48,0xbec43f6d,0x3f0e3f55,
0x3f800000,0x3f353f35,0x3f80,0xbf353f35,0xbf800000,0xbf0ebf55,0x3ec4bf6d,0x3f7bbe48,0x3f800000,0x3ec43f6d,0xbf353f35,0xbf6dbec4,0xbf800000,0xbe48bf7b,0x3f6dbec4,0x3f0e3f55,
0x3f800000,0xbf353f35,0xbf80,0x3f353f35,0xbf800000,0x3f55bf0e,0xbec43f6d,0xbe48bf7b,0x3f800000,0xbf6d3ec4,0x3f35bf35,0xbec43f6d,0xbf800000,0x3f7bbe48,0xbf6d3ec4,0x3f55bf0e,
0x3f800000,0xbf35bf35,0x3f80,0x3f35bf35,0xbf800000,0x3f0e3f55,0x3ec4bf6d,0xbf7b3e48,0x3f800000,0xbec4bf6d,0xbf353f35,0x3f6d3ec4,0xbf800000,0x3e483f7b,0x3f6dbec4,0xbf0ebf55,
0x3f800000,0x3f35bf35,0xbf80,0xbf35bf35,0xbf800000,0xbf553f0e,0xbec43f6d,0x3e483f7b,0x3f800000,0x3f6dbec4,0x3f35bf35,0x3ec4bf6d,0xbf800000,0xbf7b3e48,0xbf6d3ec4,0xbf553f0e,
0xbf800000,0xbf35bf35,0xbf80,0x3f35bf35,0x3f353f35,0xbe483f7b,0xbf6d3ec4,0xbf55bf0e,0xbf80,0x3f6dbec4,0x3f353f35,0xbec43f6d,0xbf353f35,0xbf55bf0e,0x3ec4bf6d,0x3f7b3e48,
0xbf800000,0x3f35bf35,0x3f80,0xbf35bf35,0x3f353f35,0xbf7bbe48,0x3f6dbec4,0xbf0e3f55,0xbf80,0x3ec43f6d,0xbf35bf35,0x3f6d3ec4,0xbf353f35,0x3f0ebf55,0xbec43f6d,0x3e48bf7b,
0xbf800000,0x3f353f35,0xbf80,0xbf353f35,0x3f353f35,0x3e48bf7b,0xbf6d3ec4,0x3f553f0e,0xbf80,0xbf6d3ec4,0x3f353f35,0x3ec4bf6d,0xbf353f35,0x3f553f0e,0x3ec4bf6d,0xbf7bbe48,
0xbf800000,0xbf353f35,0x3f80,0x3f353f35,0x3f353f35,0x3f7b3e48,0x3f6dbec4,0x3f0ebf55,0xbf80,0xbec4bf6d,0xbf35bf35,0xbf6dbec4,0xbf353f35,0xbf0e3f55,0xbec43f6d,0xbe483f7b,
0x3f800000,0x3f353f35,0x3f80,0xbf353f35,0xbf80,0x3f55bf0e,0x3f6d3ec4,0x3e483f7b,0xbf800000,0xbec4bf6d,0x3f35bf35,0x3f6d3ec4,0x3f80,0xbf7b3e48,0xbec4bf6d,0x3f55bf0e,
0x3f800000,0xbf353f35,0xbf80,0x3f353f35,0xbf80,0x3f0e3f55,0xbf6dbec4,0x3f7bbe48,0xbf800000,0x3f6dbec4,0xbf353f35,0x3ec4bf6d,0x3f80,0xbe48bf7b,0x3ec43f6d,0xbf0ebf55,
0x3f800000,0xbf35bf35,0x3f80,0x3f35bf35,0xbf80,0xbf553f0e,0x3f6d3ec4,0xbe48bf7b,0xbf800000,0x3ec43f6d,0x3f35bf35,0xbf6dbec4,0x3f80,0x3f7bbe48,0xbec4bf6d,0xbf553f0e,
0x3f800000,0x3f35bf35,0xbf80,0xbf35bf35,0xbf80,0xbf0ebf55,0xbf6dbec4,0xbf7b3e48,0xbf800000,0xbf6d3ec4,0xbf353f35,0xbec43f6d,0x3f80,0x3e483f7b,0x3ec43f6d,0x3f0e3f55,
0xbf800000,0xbf35bf35,0xbf80,0x3f35bf35,0xbf353f35,0xbf7bbe48,0xbec4bf6d,0x3f0ebf55,0x3f80,0xbf6d3ec4,0xbf35bf35,0x3ec4bf6d,0x3f353f35,0xbf0e3f55,0xbf6dbec4,0x3e48bf7b,
0xbf800000,0x3f35bf35,0x3f80,0xbf35bf35,0xbf353f35,0x3e48bf7b,0x3ec43f6d,0xbf55bf0e,0x3f80,0xbec4bf6d,0x3f353f35,0xbf6dbec4,0x3f353f35,0xbf55bf0e,0x3f6d3ec4,0xbf7bbe48,
0xbf800000,0x3f353f35,0xbf80,0xbf353f35,0xbf353f35,0x3f7b3e48,0xbec4bf6d,0xbf0e3f55,0x3f80,0x3f6dbec4,0xbf35bf35,0xbec43f6d,0x3f353f35,0x3f0ebf55,0xbf6dbec4,0xbe483f7b,
0xbf800000,0xbf353f35,0x3f80,0x3f353f35,0xbf353f35,0xbe483f7b,0x3ec43f6d,0x3f553f0e,0x3f80,0x3ec43f6d,0x3f353f35,0x3f6d3ec4,0x3f353f35,0x3f553f0e,0x3f6d3ec4,0x3f7b3e48,
0x80004000,0x80004000,0x80004000,0x80004000,0xbe493fff,0xbf153ff5,0xbf713fe2,0xbfa23fc6,0xbec83ffb,0xbf8e3fd5,0xbfd53f8e,0xbffb3ec8,0xbf153ff5,0xbfc63fa2,0xbfff3e49,0xbfe2bf71,
0xbfb53fb5,0xbfb5bfb5,0x3fb5bfb5,0x3fb53fb5,0xbfc63fa2,0xbf71bfe2,0x3ff5bf15,0x3e493fff,0xbfd53f8e,0xbec8bffb,0x3ffb3ec8,0xbf8e3fd5,0xbfe23f71,0x3e49bfff,0x3fc63fa2,0xbff53f15,
0xc0000000,0x40000000,0xc0000000,0x40000000,0xbfffbe49,0x3ff53f15,0xbfe2bf71,0x3fc63fa2,0xbffbbec8,0x3fd53f8e,0xbf8ebfd5,0x3ec83ffb,0xbff5bf15,0x3fa23fc6,0xbe49bfff,0xbf713fe2,
0xbfb5bfb5,0xbfb53fb5,0x3fb53fb5,0x3fb5bfb5,0xbfa2bfc6,0xbfe23f71,0x3f153ff5,0x3fffbe49,0xbf8ebfd5,0xbffb3ec8,0xbec83ffb,0x3fd53f8e,0xbf71bfe2,0xbfffbe49,0xbfa23fc6,0x3f153ff5,
0x80004000,0x80004000,0x80004000,0x80004000,0xbfc63fa2,0xbfe23f71,0xbff53f15,0xbfff3e49,0xbffbbec8,0xbfd5bf8e,0xbf8ebfd5,0xbec8bffb,0xbf71bfe2,0x3e49bfff,0x3fa2bfc6,0x3ff5bf15,
0xbfb53fb5,0xbfb5bfb5,0x3fb5bfb5,0x3fb53fb5,0xbfffbe49,0x3f15bff5,0x3fe23f71,0xbfa23fc6,0xbf8ebfd5,0x3ffbbec8,0xbec83ffb,0xbfd5bf8e,0x3f15bff5,0x3fa23fc6,0xbfff3e49,0x3f71bfe2,
0xc0000000,0x40000000,0xc0000000,0x40000000,0xbfa2bfc6,0x3f713fe2,0xbf15bff5,0x3e493fff,0x3ec8bffb,0xbf8e3fd5,0x3fd5bf8e,0xbffb3ec8,0x3fe2bf71,0xbfffbe49,0x3fc63fa2,0xbf15bff5,
0xbfb5bfb5,0xbfb53fb5,0x3fb53fb5,0x3fb5bfb5,0x3e49bfff,0xbff5bf15,0xbf713fe2,0x3fc63fa2,0x3fd5bf8e,0xbec8bffb,0xbffbbec8,0xbf8e3fd5,0x3ff53f15,0x3fc6bfa2,0xbe49bfff,0xbfe2bf71,
0x80004000,0x80004000,0x80004000,0x80004000,0xbfffbe49,0xbff5bf15,0xbfe2bf71,0xbfc6bfa2,0x3ec8bffb,0x3f8ebfd5,0x3fd5bf8e,0x3ffbbec8,0x3ff53f15,0x3fa23fc6,0x3e493fff,0xbf713fe2,
0xbfb53fb5,0xbfb5bfb5,0x3fb5bfb5,0x3fb53fb5,0xbfa2bfc6,0x3fe2bf71,0x3f153ff5,0xbfff3e49,0x3fd5bf8e,0x3ec83ffb,0xbffbbec8,0x3f8ebfd5,0x3f713fe2,0xbfffbe49,0x3fa2bfc6,0x3f153ff5,
0xc0000000,0x40000000,0xc0000000,0x40000000,0x3e49bfff,0xbf153ff5,0x3f71bfe2,0xbfa23fc6,0x3ffb3ec8,0xbfd5bf8e,0x3f8e3fd5,0xbec8bffb,0xbf153ff5,0x3fc6bfa2,0xbfff3e49,0x3fe23f71,
0xbfb5bfb5,0xbfb53fb5,0x3fb53fb5,0x3fb5bfb5,0x3fc6bfa2,0xbf71bfe2,0xbff53f15,0x3e493fff,0x3f8e3fd5,0x3ffbbec8,0x3ec8bffb,0xbfd5bf8e,0xbfe23f71,0xbe493fff,0x3fc63fa2,0x3ff5bf15,
0x80004000,0x80004000,0x80004000,0x80004000,0xbfa2bfc6,0xbf71bfe2,0xbf15bff5,0xbe49bfff,0x3ffb3ec8,0x3fd53f8e,0x3f8e3fd5,0x3ec83ffb,0xbfe23f71,0xbfffbe49,0xbfc6bfa2,0xbf15bff5,
0xbfb53fb5,0xbfb5bfb5,0x3fb5bfb5,0x3fb53fb5,0x3e49bfff,0x3ff53f15,0xbf713fe2,0xbfc6bfa2,0x3f8e3fd5,0xbffb3ec8,0x3ec8bffb,0x3fd53f8e,0xbff5bf15,0x3fc6bfa2,0x3e493fff,0xbfe2bf71,
0xc0000000,0x40000000,0xc0000000,0x40000000,0x3fc6bfa2,0xbfe23f71,0x3ff5bf15,0xbfff3e49,0xbec83ffb,0x3f8ebfd5,0xbfd53f8e,0x3ffbbec8,0xbf71bfe2,0xbe493fff,0x3fa2bfc6,0xbff53f15,
0xbfb5bfb5,0xbfb53fb5,0x3fb53fb5,0x3fb5bfb5,0x3fff3e49,0x3f15bff5,0xbfe2bf71,0xbfa23fc6,0xbfd53f8e,0x3ec83ffb,0x3ffb3ec8,0x3f8ebfd5,0x3f15bff5,0xbfa2bfc6,0xbfff3e49,0xbf713fe2,
0xbf443fed,0xbfed3f44,0xbfedbf44,0xbf44bfed,0xbf713fe2,0xbfff3e49,0xbfa2bfc6,0x3f15bff5,0xbf8e3fd5,0xbffbbec8,0xbec8bffb,0x3fd5bf8e,0xbfa23fc6,0xbfe2bf71,0x3f15bff5,0x3fff3e49,
0xbfed3f44,0x3f44bfed,0x3f443fed,0xbfedbf44,0xbff53f15,0x3fa2bfc6,0xbe493fff,0xbf71bfe2,0xbffb3ec8,0x3fd5bf8e,0xbf8e3fd5,0x3ec8bffb,0xbfff3e49,0x3ff5bf15,0xbfe23f71,0x3fc6bfa2,
0xbfedbf44,0x3f443fed,0x3f44bfed,0xbfed3f44,0xbfe2bf71,0x3e493fff,0x3fc6bfa2,0xbff5bf15,0xbfd5bf8e,0xbec83ffb,0x3ffbbec8,0xbf8ebfd5,0xbfc6bfa2,0xbf713fe2,0x3ff53f15,0x3e49bfff,
0xbf44bfed,0xbfedbf44,0xbfed3f44,0xbf443fed,0xbf15bff5,0xbfc6bfa2,0xbfffbe49,0xbfe23f71,0xbec8bffb,0xbf8ebfd5,0xbfd5bf8e,0xbffbbec8,0xbe49bfff,0xbf15bff5,0xbf71bfe2,0xbfa2bfc6,
0x3f44bfed,0x3fedbf44,0x3fed3f44,0x3f443fed,0x3ff5bf15,0x3fc63fa2,0xbe493fff,0xbfe23f71,0x3fd53f8e,0xbec83ffb,0xbffb3ec8,0xbf8ebfd5,0x3e493fff,0xbff53f15,0xbf71bfe2,0x3fc6bfa2,
0x3fedbf44,0xbf443fed,0xbf44bfed,0x3fed3f44,0x3fe23f71,0xbfff3e49,0x3fc6bfa2,0xbf153ff5,0x3ec83ffb,0xbf8ebfd5,0x3fd53f8e,0xbffbbec8,0xbfa23fc6,0x3f71bfe2,0xbf153ff5,0x3e49bfff,
0x3fed3f44,0xbf44bfed,0xbf443fed,0x3fedbf44,0x3f153ff5,0x3fa2bfc6,0xbfffbe49,0x3f713fe2,0xbf8e3fd5,0x3ffb3ec8,0xbec8bffb,0xbfd53f8e,0xbfff3e49,0x3f153ff5,0x3fe2bf71,0xbfa2bfc6,
0x3f443fed,0x3fed3f44,0x3fedbf44,0x3f44bfed,0xbf713fe2,0x3e493fff,0x3fa23fc6,0x3ff53f15,0xbffb3ec8,0xbfd53f8e,0xbf8e3fd5,0xbec83ffb,0xbfc6bfa2,0xbfe2bf71,0xbff5bf15,0xbfffbe49,
0xbf443fed,0xbfed3f44,0xbfedbf44,0xbf44bfed,0xbfe2bf71,0xbe49bfff,0x3fc6bfa2,0x3ff53f15,0x3f8ebfd5,0x3ffb3ec8,0x3ec83ffb,0xbfd53f8e,0x3fc63fa2,0xbf713fe2,0xbff5bf15,0x3e49bfff,
0xbfed3f44,0x3f44bfed,0x3f443fed,0xbfedbf44,0xbf15bff5,0x3fc63fa2,0xbfffbe49,0x3fe2bf71,0x3ffbbec8,0xbfd53f8e,0x3f8ebfd5,0xbec83ffb,0x3e493fff,0xbf15bff5,0x3f713fe2,0xbfa2bfc6,
0xbfedbf44,0x3f443fed,0x3f44bfed,0xbfed3f44,0x3f71bfe2,0xbfff3e49,0x3fa23fc6,0x3f15bff5,0x3fd53f8e,0x3ec8bffb,0xbffb3ec8,0x3f8e3fd5,0xbfa23fc6,0x3fe23f71,0x3f15bff5,0xbfffbe49,
0xbf44bfed,0xbfedbf44,0xbfed3f44,0xbf443fed,0x3ff5bf15,0x3fa2bfc6,0x3e49bfff,0xbf71bfe2,0x3ec83ffb,0x3f8e3fd5,0x3fd53f8e,0x3ffb3ec8,0xbfff3e49,0xbff53f15,0xbfe23f71,0xbfc63fa2,
0x3f44bfed,0x3fedbf44,0x3fed3f44,0x3f443fed,0x3f153ff5,0xbfa23fc6,0xbfffbe49,0xbf71bfe2,0xbfd5bf8e,0x3ec8bffb,0x3ffbbec8,0x3f8e3fd5,0x3fffbe49,0x3f153ff5,0xbfe23f71,0xbfa2bfc6,
0x3fedbf44,0xbf443fed,0xbf44bfed,0x3fed3f44,0xbf713fe2,0xbe49bfff,0x3fa23fc6,0xbff5bf15,0xbec8bffb,0x3f8e3fd5,0xbfd5bf8e,0x3ffb3ec8,0x3fc63fa2,0xbfe2bf71,0x3ff53f15,0xbfffbe49,
0x3fed3f44,0xbf44bfed,0xbf443fed,0x3fedbf44,0xbff53f15,0x3fc63fa2,0x3e49bfff,0xbfe23f71,0x3f8ebfd5,0xbffbbec8,0x3ec83ffb,0x3fd5bf8e,0x3e493fff,0x3ff5bf15,0xbf71bfe2,0xbfc63fa2,
0x3f443fed,0x3fed3f44,0x3fedbf44,0x3f44bfed,0xbfe2bf71,0xbfff3e49,0xbfc63fa2,0xbf153ff5,0x3ffbbec8,0x3fd5bf8e,0x3f8ebfd5,0x3ec8bffb,0xbfa23fc6,0xbf713fe2,0xbf153ff5,0xbe493fff,
0x40003f80,0x40004000,0x80003f80,0x80008000,0x3ffb3f80,0x3fd53fed,0xbec8bf80,0xbf8ebf44,0x3fed3f80,0x3f443fb5,0xbf443f80,0xbfedbfb5,0x3fd53f80,0xbec83f44,0xbf8ebf80,0xbffbbfed,
0x3fb53f80,0xbfb50000,0xbfb53f80,0xbfb5c000,0x3f8e3f80,0xbffbbf44,0xbfd5bf80,0xbec8bfed,0x3f443f80,0xbfedbfb5,0xbfed3f80,0x3f44bfb5,0x3ec83f80,0xbf8ebfed,0xbffbbf80,0x3fd5bf44,
0x3f80,0xc000,0xc0003f80,0x40008000,0xbec83f80,0x3f8ebfed,0xbffbbf80,0x3fd53f44,0xbf443f80,0x3fedbfb5,0xbfed3f80,0x3f443fb5,0xbf8e3f80,0x3ffbbf44,0xbfd5bf80,0xbec83fed,
0xbfb53f80,0x3fb50000,0xbfb53f80,0xbfb54000,0xbfd53f80,0x3ec83f44,0xbf8ebf80,0xbffb3fed,0xbfed3f80,0xbf443fb5,0xbf443f80,0xbfed3fb5,0xbffb3f80,0xbfd53fed,0xbec8bf80,0xbf8e3f44,
0x40004000,0x40004000,0x80008000,0x80008000,0x3f8e3fb5,0x3ec83f44,0xbfd5bfb5,0xbffbbfed,0xbf440000,0xbfedbfb5,0xbfedc000,0xbf44bfb5,0xbffbbfb5,0xbf8ebfed,0xbec8bfb5,0x3fd53f44,
0xbfb5c000,0x3fb50000,0x3fb58000,0x3fb54000,0x3ec8bfb5,0x3fd53fed,0x3ffb3fb5,0xbf8e3f44,0x3fed0000,0xbf443fb5,0x3f444000,0xbfedbfb5,0x3fd53fb5,0xbffbbf44,0xbf8e3fb5,0x3ec8bfed,
0x4000,0xc000,0xc0008000,0x40008000,0xbfd53fb5,0x3ffbbf44,0xbf8ebfb5,0x3ec83fed,0xbfed0000,0x3f443fb5,0x3f44c000,0xbfed3fb5,0xbec8bfb5,0xbfd53fed,0x3ffbbfb5,0xbf8ebf44,
0x3fb5c000,0xbfb50000,0x3fb58000,0x3fb5c000,0x3ffbbfb5,0x3f8ebfed,0xbec83fb5,0x3fd5bf44,0x3f440000,0x3fedbfb5,0xbfed4000,0xbf443fb5,0xbf8e3fb5,0xbec83f44,0xbfd53fb5,0xbffb3fed,
0x40004000,0x40004000,0x80008000,0x80008000,0xbec80000,0xbf8ebf44,0xbffbc000,0xbfd5bfed,0xbfedc000,0xbf44bfb5,0x3f448000,0x3fed3fb5,0x3f8e0000,0x3ffb3fed,0x3fd54000,0xbec83f44,
0x3fb54000,0xbfb50000,0xbfb58000,0xbfb5c000,0xbfd50000,0xbec8bfed,0xbf8ec000,0x3ffb3f44,0xbf44c000,0x3fed3fb5,0x3fed8000,0xbf443fb5,0x3ffb0000,0xbfd53f44,0x3ec84000,0xbf8ebfed,
0x4000,0xc000,0xc0008000,0x40008000,0xbffb0000,0x3fd53f44,0x3ec8c000,0xbf8e3fed,0x3f44c000,0xbfed3fb5,0x3fed8000,0xbf44bfb5,0x3fd50000,0x3ec8bfed,0xbf8e4000,0x3ffbbf44,
0xbfb54000,0x3fb50000,0xbfb58000,0xbfb54000,0xbf8e0000,0xbffb3fed,0x3fd5c000,0xbec8bf44,0x3fedc000,0x3f44bfb5,0x3f448000,0x3fedbfb5,0x3ec80000,0x3f8ebf44,0xbffb4000,0xbfd53fed,
0x40004000,0x40004000,0x80008000,0x80008000,0xbfd5bfb5,0xbffbbfed,0xbf8ebfb5,0xbec8bf44,0x3f440000,0x3fed3fb5,0x3fed4000,0x3f443fb5,0x3ec83fb5,0xbfd5bf44,0xbffbbfb5,0xbf8ebfed,
0xbfb5c000,0x3fb50000,0x3fb58000,0x3fb54000,0x3ffb3fb5,0xbf8e3f44,0xbec83fb5,0xbfd5bfed,0xbfed0000,0x3f44bfb5,0xbf44c000,0x3fed3fb5,0x3f8ebfb5,0xbec83fed,0x3fd53fb5,0xbffbbf44,
0x4000,0xc000,0xc0008000,0x40008000,0xbf8ebfb5,0x3ec83fed,0x3fd5bfb5,0xbffb3f44,0x3fed0000,0xbf44bfb5,0xbf444000,0x3fedbfb5,0xbffb3fb5,0x3f8e3f44,0xbec8bfb5,0xbfd53fed,
0x3fb5c000,0xbfb50000,0x3fb58000,0x3fb5c000,0xbec83fb5,0x3fd5bf44,0xbffb3fb5,0xbf8e3fed,0xbf440000,0xbfed3fb5,0x3fedc000,0x3f44bfb5,0x3fd5bfb5,0x3ffbbfed,0xbf8e3fb5,0xbec83f44,
0xc0003f80,0xc0004000,0x80003f80,0x80008000,0xbffb3f80,0xbfd53fed,0x3ec8bf80,0x3f8ebf44,0xbfed3f80,0xbf443fb5,0x3f443f80,0x3fedbfb5,0xbfd53f80,0x3ec83f44,0x3f8ebf80,0x3ffbbfed,
0xbfb53f80,0x3fb50000,0x3fb53f80,0x3fb5c000,0xbf8e3f80,0x3ffbbf44,0x3fd5bf80,0x3ec8bfed,0xbf443f80,0x3fedbfb5,0x3fed3f80,0xbf44bfb5,0xbec83f80,0x3f8ebfed,0x3ffbbf80,0xbfd5bf44,
0x3f80,0xc000,0x40003f80,0xc0008000,0x3ec83f80,0xbf8ebfed,0x3ffbbf80,0xbfd53f44,0x3f443f80,0xbfedbfb5,0x3fed3f80,0xbf443fb5,0x3f8e3f80,0xbffbbf44,0x3fd5bf80,0x3ec83fed,
0x3fb53f80,0xbfb50000,0x3fb53f80,0x3fb54000,0x3fd53f80,0xbec83f44,0x3f8ebf80,0x3ffb3fed,0x3fed3f80,0x3f443fb5,0x3f443f80,0x3fed3fb5,0x3ffb3f80,0x3fd53fed,0x3ec8bf80,0x3f8e3f44,
0xc0004000,0xc0004000,0x80008000,0x80008000,0xbf8e3fb5,0xbec83f44,0x3fd5bfb5,0x3ffbbfed,0x3f440000,0x3fedbfb5,0x3fedc000,0x3f44bfb5,0x3ffbbfb5,0x3f8ebfed,0x3ec8bfb5,0xbfd53f44,
0x3fb5c000,0xbfb50000,0xbfb58000,0xbfb54000,0xbec8bfb5,0xbfd53fed,0xbffb3fb5,0x3f8e3f44,0xbfed0000,0x3f443fb5,0xbf444000,0x3fedbfb5,0xbfd53fb5,0x3ffbbf44,0x3f8e3fb5,0xbec8bfed,
0x4000,0xc000,0x40008000,0xc0008000,0x3fd53fb5,0xbffbbf44,0x3f8ebfb5,0xbec83fed,0x3fed0000,0xbf443fb5,0xbf44c000,0x3fed3fb5,0x3ec8bfb5,0x3fd53fed,0xbffbbfb5,0x3f8ebf44,
0xbfb5c000,0x3fb50000,0xbfb58000,0xbfb5c000,0xbffbbfb5,0xbf8ebfed,0x3ec83fb5,0xbfd5bf44,0xbf440000,0xbfedbfb5,0x3fed4000,0x3f443fb5,0x3f8e3fb5,0x3ec83f44,0x3fd53fb5,0x3ffb3fed,
0xc0004000,0xc0004000,0x80008000,0x80008000,0x3ec80000,0x3f8ebf44,0x3ffbc000,0x3fd5bfed,0x3fedc000,0x3f44bfb5,0xbf448000,0xbfed3fb5,0xbf8e0000,0xbffb3fed,0xbfd54000,0x3ec83f44,
0xbfb54000,0x3fb50000,0x3fb58000,0x3fb5c000,0x3fd50000,0x3ec8bfed,0x3f8ec000,0xbffb3f44,0x3f44c000,0xbfed3fb5,0xbfed8000,0x3f443fb5,0xbffb0000,0x3fd53f44,0xbec84000,0x3f8ebfed,
0x4000,0xc000,0x40008000,0xc0008000,0x3ffb0000,0xbfd53f44,0xbec8c000,0x3f8e3fed,0xbf44c000,0x3fed3fb5,0xbfed8000,0x3f44bfb5,0xbfd50000,0xbec8bfed,0x3f8e4000,0xbffbbf44,
0x3fb54000,0xbfb50000,0x3fb58000,0x3fb54000,0x3f8e0000,0x3ffb3fed,0xbfd5c000,0x3ec8bf44,0xbfedc000,0xbf44bfb5,0xbf448000,0xbfedbfb5,0xbec80000,0xbf8ebf44,0x3ffb4000,0x3fd53fed,
0xc0004000,0xc0004000,0x80008000,0x80008000,0x3fd5bfb5,0x3ffbbfed,0x3f8ebfb5,0x3ec8bf44,0xbf440000,0xbfed3fb5,0xbfed4000,0xbf443fb5,0xbec83fb5,0x3fd5bf44,0x3ffbbfb5,0x3f8ebfed,
0x3fb5c000,0xbfb50000,0xbfb58000,0xbfb54000,0xbffb3fb5,0x3f8e3f44,0x3ec83fb5,0x3fd5bfed,0x3fed0000,0xbf44bfb5,0x3f44c000,0xbfed3fb5,0xbf8ebfb5,0x3ec83fed,0xbfd53fb5,0x3ffbbf44,
0x4000,0xc000,0x40008000,0xc0008000,0x3f8ebfb5,0xbec83fed,0xbfd5bfb5,0x3ffb3f44,0xbfed0000,0x3f44bfb5,0x3f444000,0xbfedbfb5,0x3ffb3fb5,0xbf8e3f44,0x3ec8bfb5,0x3fd53fed,
0xbfb5c000,0x3fb50000,0xbfb58000,0xbfb5c000,0x3ec83fb5,0xbfd5bf44,0x3ffb3fb5,0x3f8e3fed,0x3f440000,0x3fed3fb5,0xbfedc000,0xbf44bfb5,0xbfd5bfb5,0xbffbbfed,0x3f8e3fb5,0x3ec83f44,
};

#define STAMP_DECL
#define CLK(n)
#define STAMP(kid, n)
#define STAMPT(kid, n, t)
#define STAMP_END(kid, n)

DI float bflo(unsigned v) { return __uint_as_float(v << 16); }
DI float bfhi(unsigned v) { return __uint_as_float(v & 0xffff0000u); }
DI unsigned pk_bf16(float a, float b) { f32x2 v = {a, b}; return __builtin_bit_cast(unsigned, __builtin_convertvector(v, b16x2)); }
DI unsigned pk_f16(float a, float b) { f32x2 v = {a, b}; return __builtin_bit_cast(unsigned, __builtin_convertvector(v, h16x2)); }
DI float rbf(float a) { return bflo(pk_bf16(a, a)); }
DI float sinr(float r) { return __builtin_amdgcn_sinf(r); }
DI float cosr(float r) { return __builtin_amdgcn_cosf(r); }
DI void glds16(const void* src, LAS void* dst) { __builtin_amdgcn_global_load_lds((const GAS void*)src, dst, 16, 0, 0); }
template <int N> DI void wait_vm() { asm volatile("s_waitcnt vmcnt(%0)" ::"i"(N) : "memory"); }
DI f32x4 mfma_h(u32x4 a, u32x4 b, f32x4 c) { return __builtin_amdgcn_mfma_f32_16x16x32_f16(__builtin_bit_cast(h16x8, a), __builtin_bit_cast(h16x8, b), c, 0, 0, 0); }
DI f32x4 mfma_b(u32x4 a, u32x4 b, f32x4 c) { return __builtin_amdgcn_mfma_f32_16x16x32_bf16(__builtin_bit_cast(b16x8, a), __builtin_bit_cast(b16x8, b), c, 0, 0, 0); }
DI f32x4 mfma16_h(u32x2 a, u32x2 b, f32x4 c) { return __builtin_amdgcn_mfma_f32_16x16x16f16(__builtin_bit_cast(h16x4, a), __builtin_bit_cast(h16x4, b), c, 0, 0, 0); }
DI f32x4 mfma16_b(u32x2 a, u32x2 b, f32x4 c) { return __builtin_amdgcn_mfma_f32_16x16x16bf16_1k(__builtin_bit_cast(s16x4, a), __builtin_bit_cast(s16x4, b), c, 0, 0, 0); }
DI u32x2 tr_read(const LAS char* p) { return __builtin_bit_cast(u32x2, __builtin_amdgcn_ds_read_tr16_b64_v4i16((LAS s16x4*)p)); }
typedef __amdgpu_buffer_rsrc_t rsrc_t;
DI rsrc_t mk_rsrc(const void* p, unsigned bytes) { return __builtin_amdgcn_make_buffer_rsrc((void*)p, 0, bytes, 0x00020000); }
#define ST_AUX 16
DI void bst128(rsrc_t r, unsigned off, u32x4 v) { __builtin_amdgcn_raw_buffer_store_b128(v, r, off, 0, ST_AUX); }
DI void bst64(rsrc_t r, unsigned off, u32x2 v) { __builtin_amdgcn_raw_buffer_store_b64(v, r, off, 0, ST_AUX); }
DI void bst32(rsrc_t r, unsigned off, unsigned v) { __builtin_amdgcn_raw_buffer_store_b32(v, r, off, 0, ST_AUX); }
template <class T> DI T ldg(const void* base, unsigned off) { return *(const T*)((const char*)base + off); }
#define PIN(x) asm volatile("" : "+v"(x))
DI f32x2 swp(f32x2 v) { return __builtin_shufflevector(v, v, 1, 0); }
DI f32x2 lo2(f32x4 v) { return __builtin_shufflevector(v, v, 0, 1); }
DI f32x2 hi2(f32x4 v) { return __builtin_shufflevector(v, v, 2, 3); }
DI unsigned pk_bf16v(f32x2 v) { return __builtin_bit_cast(unsigned, __builtin_convertvector(v, b16x2)); }
DI unsigned pk_f16v(f32x2 v) { return __builtin_bit_cast(unsigned, __builtin_convertvector(v, h16x2)); }
DI f32x2 unbf(unsigned v) { return (f32x2){bflo(v), bfhi(v)}; }
template <int CTRL> DI float dpp_add(float v) { return v + __int_as_float(__builtin_amdgcn_update_dpp(0, __float_as_int(v), CTRL, 0xf, 0xf, true)); }
DI float wave_sum(float v) {
  v = dpp_add<0xB1>(v); v = dpp_add<0x4E>(v); v = dpp_add<0x141>(v); v = dpp_add<0x140>(v);
  const int i = __float_as_int(v);
  return (__int_as_float(__builtin_amdgcn_readlane(i, 0)) + __int_as_float(__builtin_amdgcn_readlane(i, 16))) +
         (__int_as_float(__builtin_amdgcn_readlane(i, 32)) + __int_as_float(__builtin_amdgcn_readlane(i, 48)));
}
template <int CTRL> DI f32x2 dpp_add2(f32x2 v) { return (f32x2){dpp_add<CTRL>(v.x), dpp_add<CTRL>(v.y)}; }
DI float lane_f(float v, int l) { return __int_as_float(__builtin_amdgcn_readlane(__float_as_int(v), l)); }
template <bool RMEAN>
DI void ln_stats(const f32x2 (&x)[8], float& rden, float& nm) {
  const float x0 = lane_f(x[0].x, 0);
  f32x2 s = {0.f, 0.f}, q = {0.f, 0.f};
#pragma unroll
  for (int i = 0; i < 8; ++i) { const f32x2 d = x[i] - x0; s += d; q += d * d; }
  f32x2 v = {s.x + s.y, q.x + q.y};
  v = dpp_add2<0xB1>(v); v = dpp_add2<0x4E>(v); v = dpp_add2<0x141>(v); v = dpp_add2<0x140>(v);
  const float md = ((lane_f(v.x, 0) + lane_f(v.x, 16)) + (lane_f(v.x, 32) + lane_f(v.x, 48))) * (1.0f / 1024.0f);
  const float m2 = ((lane_f(v.y, 0) + lane_f(v.y, 16)) + (lane_f(v.y, 32) + lane_f(v.y, 48))) * (1.0f / 1024.0f);
  const float var = __builtin_fmaxf(m2 - md * md, 0.f);
  rden = __builtin_amdgcn_rcpf(rbf(__builtin_amdgcn_sqrtf(rbf(rbf(var) + 1e-5f))));
  nm = -(RMEAN ? rbf(x0 + md) : x0 + md) * rden;
}
DI void glds_asm(unsigned voff, const void* base, unsigned ldsaddr) {
  asm volatile("s_mov_b32 m0, %2\n\ts_nop 3\n\tglobal_load_lds_dwordx4 %0, %1" ::"v"(voff), "s"(base), "s"(ldsaddr) : "memory");
}
DI void glds_asm_sc1(unsigned voff, const void* base, unsigned ldsaddr) {
  asm volatile("s_mov_b32 m0, %2\n\ts_nop 3\n\tglobal_load_lds_dwordx4 %0, %1 sc1" ::"v"(voff), "s"(base), "s"(ldsaddr) : "memory");
}
DI int opaque(int v) { asm volatile("" : "+v"(v)); return v; }
DI int sopaque(int v) { asm volatile("" : "+s"(v)); return v; }
#define LANE_VARS                                                                                                              \
  const int lane = opaque(threadIdx.x) & 63, x16 = lane & 15, g = lane >> 4;                                                   \
  const unsigned co = x16 * 64 + g * 16;                                                                                       \
  const int w = sopaque(w_), blk = sopaque(blockIdx.x), LAYER = sopaque(layer_);                                               \
  const int xcd = blk & 7, loc = blk >> 3;                                                                                     \
  const int panel = (xcd & 1) * 8 + (loc & 7), idx = (xcd >> 1) * 4 + (loc >> 3), b = 16 * panel + idx;                        \
  const int split = idx >> 3, j2 = idx & 7;                                                                                    \
  unsigned* cnt = B.cnt + LAYER * NCNT * 32;                                                                                   \
  (void)co; (void)x16; (void)g; (void)b; (void)split; (void)j2; (void)cnt; (void)w
DI void bar() { asm volatile("" ::: "memory"); __builtin_amdgcn_s_barrier(); asm volatile("" ::: "memory"); }

DI void lds_barrier() { asm volatile("s_waitcnt lgkmcnt(0)" ::: "memory"); __builtin_amdgcn_s_barrier(); asm volatile("" ::: "memory"); }
constexpr int NCNT = 160;
constexpr int WARM_LINES = 208;
constexpr int NPREP = 224;

template <int WHICH>
DI void prep_load(const u16* __restrict__ W, int task, unsigned ldsaddr, int lane) {
  constexpr int N = WHICH == 0 ? 3584 : 1024, NG = N / 64;
  const int ks = task / NG, cg = task % NG;
  const unsigned src = ((ks * 32 + (lane >> 3)) * N + cg * 64 + (lane & 7) * 8) * 2;
#pragma unroll
  for (int i = 0; i < 4; ++i) glds_asm(src + i * 16 * N, W, ldsaddr + i * 1024);
}
template <int WHICH>
DI void prep_store(u16* __restrict__ Wt, int task, const LAS char* lds, int lane) {
  constexpr int NG = (WHICH == 0 ? 3584 : 1024) / 64;
  const int ks = task / NG, cg = task % NG;
  const int g = lane >> 4, q = (lane & 15) >> 2, p = lane & 3;
#pragma unroll
  for (int f = 0; f < 4; ++f) {
    u32x2 lo, hi; unsigned dst;
    if (WHICH == 0) {
      lo = tr_read(lds + (8 * g + q) * 128 + f * 32 + p * 8);
      hi = tr_read(lds + (8 * g + 4 + q) * 128 + f * 32 + p * 8);
      const int ct = cg * 4 + f, j = ct / 14, nt = ct % 14;
      dst = ((j * 32 + ks) * 14 + nt) * 1024;
    } else {
      const int co = (f >> 1) * 64 + p * 16 + (f & 1) * 8;
      lo = tr_read(lds + (4 * g + q) * 128 + co);
      hi = tr_read(lds + (16 + 4 * g + q) * 128 + co);
      dst = (((cg >> 1) * 112 + ks) * 8 + (cg & 1) * 4 + f) * 1024;
    }
    bst128(mk_rsrc(Wt, 7340032), dst + lane * 16, (u32x4){lo.x, lo.y, hi.x, hi.y});
  }
}

DI int e_addr(int k, int s1) { const int sg = (k & 3) ^ (((k >> 5) & 1) << 1); return (((k >> 6) * 64 + (k & 3) * 16 + ((k >> 2) & 15)) * 32) + (((s1 >> 1) ^ sg) * 8) + (s1 & 1) * 4; }
struct FwdPre { f32x2 tc[4], ts[4]; u32x2 M; int ea[2][2], en, e16[2], va, ra[2], er; unsigned oo; };
DI void fwd_pre(FwdPre& P, int lane, int w, int b) {
  const int x16 = lane & 15, g = lane >> 4;
#pragma unroll
  for (int i = 0; i < 8; ++i) { const float a = (float)((16 * (i >> 2) + 4 * g + (i & 3)) * x16) * (1.0f / 1024.0f); P.tc[i >> 1][i & 1] = cosr(a); P.ts[i >> 1][i & 1] = sinr(a); }
  const int u1 = x16 >> 1, op = x16 & 1;
  const float a0 = (float)((u1 * (256 * (2 * g) + b)) & 2047) * (1.0f / 2048.0f), a1 = (float)((u1 * (256 * (2 * g + 1) + b)) & 2047) * (1.0f / 2048.0f);
  const float c0 = cosr(a0) * 0.125f, s0 = sinr(a0) * 0.125f, c1 = cosr(a1) * 0.125f, s1 = sinr(a1) * 0.125f;
  P.M.x = op ? pk_f16(-s0, c0) : pk_f16(c0, s0);
  P.M.y = op ? pk_f16(-s1, c1) : pk_f16(c1, s1);
#pragma unroll
  for (int j = 0; j < 2; ++j) { P.ea[0][j] = e_addr(x16 + 32 * (4 * g + j + 2) - 1, w) - 2048; P.ea[1][j] = e_addr(511 - x16 - 32 * (4 * g + j + 2), w); P.e16[j] = e_addr(15 + 32 * (4 * g + j), w); }
  P.en = lane == 0 ? 16384 + w * 4 : P.ea[0][0];
  P.va = w * 2048 + x16 * 64 + ((g ^ (((x16 >> 1) ^ ((x16 >> 3) << 1)) & 3)) * 16);
#pragma unroll
  for (int t = 0; t < 2; ++t) { const int n1 = 8 * g + 4 * t + (x16 >> 2), f = ((n1 >> 1) ^ ((n1 >> 3) << 1)) & 3; P.ra[t] = w * 2048 + n1 * 64 + ((((lane >> 1) & 1) ^ f) * 16) + (lane & 1) * 8; }
  P.er = (64 * w + x16) * 32 + ((g ^ ((x16 >> 3) << 1)) * 8);
  P.oo = (2 * g) * 524288 + (4 * w + (x16 >> 2)) * 16384 + b * 64 + (x16 & 3) * 16;
  PIN(P.er); PIN(P.oo);
#pragma unroll
  for (int i = 0; i < 4; ++i) { PIN(P.tc[i]); PIN(P.ts[i]); }
  PIN(P.M); PIN(P.en); PIN(P.ea[0][0]); PIN(P.ea[0][1]); PIN(P.ea[1][0]); PIN(P.ea[1][1]); PIN(P.e16[0]); PIN(P.e16[1]); PIN(P.va); PIN(P.ra[0]); PIN(P.ra[1]);
}
DI void fwd_main(const FwdPre& P, const f32x2 (&x)[8], const u32x4 (&T)[8], LAS char* V, LAS char* E, unsigned* __restrict__ XF, int lane, int w, int b) {
  const int x16 = lane & 15, g = lane >> 4;
  float rden, nm;
  ln_stats<false>(x, rden, nm);
#pragma unroll
  for (int i = 0; i < 2; ++i) {
    u32x4 o;
#pragma unroll
    for (int e = 0; e < 4; ++e) o[e] = pk_f16v(x[4 * i + e] * rden + nm);
    *(LAS u32x4*)(V + P.va + 1024 * i) = o;
  }
  asm volatile("s_waitcnt lgkmcnt(0)" ::: "memory");
  const f32x4 zero = {0.f, 0.f, 0.f, 0.f};
  f32x4 re[2], im[2];
  {
    const u32x4 Fc = T[0], Fs = T[1];
#pragma unroll
    for (int c = 0; c < 2; ++c) {
      const u32x2 lo = tr_read(V + (P.ra[0] ^ (c * 32))), hi = tr_read(V + (P.ra[1] ^ (c * 32)));
      const u32x4 a = {lo.x, lo.y, hi.x, hi.y};
      re[c] = mfma_h(a, Fc, zero); im[c] = mfma_h(a, Fs, zero);
    }
  }
  u32x4 Br, Bi, B16, Bn;
#pragma unroll
  for (int h = 0; h < 4; ++h) {
    const f32x2 r = (h & 1) ? hi2(re[h >> 1]) : lo2(re[h >> 1]), i = (h & 1) ? hi2(im[h >> 1]) : lo2(im[h >> 1]);
    Br[h] = pk_f16v(r * P.tc[h] + i * P.ts[h]); B16[h] = pk_f16v(i * P.tc[h] - r * P.ts[h]);
    Bi[h] = x16 == 0 ? 0u : B16[h]; Bn[h] = Br[h] ^ 0x80008000u;
  }
  {
    const u32x4 Cm = T[2], Sm = T[4];
    f32x4 xr = mfma_h(Cm, Br, zero); xr = mfma_h(Sm, Bi, xr);
    f32x4 xi = mfma_h(Cm, Bi, zero); xi = mfma_h(Sm, Bn, xi);
#pragma unroll
    for (int j = 0; j < 4; ++j) *(LAS unsigned*)(E + (j == 0 ? P.en : P.ea[0][j & 1] + (j >> 1) * 2048)) = pk_f16(xr[j], xi[j]);
  }
  {
    const u32x4 Cm = T[3], Sm = T[5];
    f32x4 xr = mfma_h(Cm, Br, zero); xr = mfma_h(Sm, Bi, xr);
    f32x4 xi = mfma_h(Cm, Bi, zero); xi = mfma_h(Sm, Bn, xi);
#pragma unroll
    for (int j = 0; j < 4; ++j) *(LAS unsigned*)(E + P.ea[1][j & 1] + (1 - (j >> 1)) * 2048) = pk_f16(xr[j], -xi[j]);
  }
  {
    const f32x4 xr = mfma_h(T[6], B16, zero), xi = mfma_h(T[7], B16, zero);
    if (x16 == 0) {
#pragma unroll
      for (int j = 0; j < 4; ++j) *(LAS unsigned*)(E + P.e16[j & 1] + (j >> 1) * 2048) = pk_f16(xr[j], xi[j]);
    }
  }
  lds_barrier();
  u32x4 o0, o1;
#pragma unroll
  for (int t = 0; t < 4; ++t) {
    const u32x2 d = *(const LAS u32x2*)(E + (P.er ^ (t * 8)) + t * 512);
    const f32x4 r = mfma16_h(P.M, d, zero);
    o0[t] = pk_f16v(lo2(r)); o1[t] = pk_f16v(hi2(r));
  }
  const rsrc_t rf = mk_rsrc(XF, 4194304);
  bst128(rf, P.oo, o0);
  bst128(rf, P.oo + 524288, o1);
}

__global__ __launch_bounds__(512) void k_fwd(const u16* __restrict__ xin, unsigned* __restrict__ XF, const u16* __restrict__ Wsrc, u16* __restrict__ Wdst, unsigned* __restrict__ cnt) {
  __shared__ __attribute__((aligned(1024))) char smem[8192 + 16384 + 16384 + 64];
  LAS char* lds = (LAS char*)smem;
  const int lane = threadIdx.x & 63, w = __builtin_amdgcn_readfirstlane(threadIdx.x >> 6), b = (int)blockIdx.x - 8;
  STAMP_DECL;
  STAMP(0, 0);
  if (blockIdx.x < 8) {
    const char* pc;
    asm volatile("s_getpc_b64 %0" : "=s"(pc));
    const char* src = threadIdx.x < 208 ? (const char*)g_tab + threadIdx.x * 128 : (const char*)((size_t)pc & ~(size_t)255) + (threadIdx.x - 208) * 128;
    unsigned touch = 0;
    if (threadIdx.x < 208 + WARM_LINES) touch = *(const volatile unsigned*)src;
    asm volatile("" ::"v"(touch));
    STAMP_END(0, 7);
    return;
  }
  if (b >= 256) {
    prep_load<0>(Wsrc, (b - 256) * 8 + w, (unsigned)(size_t)lds + w * 4096, lane);
    wait_vm<0>();
    prep_store<0>(Wdst, (b - 256) * 8 + w, lds + w * 4096, lane);
    STAMP_END(0, 7);
    return;
  }
  const unsigned ro = (256 * w + b) * 2048 + (lane & 15) * 64 + (lane >> 4) * 16;
  const u32x4 tab = ldg<u32x4>(g_tab, w * 1024 + lane * 16);
  const u32x4 in0 = ldg<u32x4>(xin, ro), in1 = ldg<u32x4>(xin, ro + 1024);
  if (b < 20) bst32(mk_rsrc(cnt, 2 * NCNT * 128), (b * 512 + threadIdx.x) * 4, 0u);
  FwdPre P;
  fwd_pre(P, lane, w, b);
  *(LAS u32x4*)(lds + w * 1024 + lane * 16) = tab;
  lds_barrier();
  u32x4 T[8];
#pragma unroll
  for (int i = 0; i < 8; ++i) { T[i] = *(const LAS u32x4*)(lds + i * 1024 + lane * 16); PIN(T[i]); }
  f32x2 x[8];
#pragma unroll
  for (int i = 0; i < 4; ++i) { x[i] = unbf(in0[i]); x[4 + i] = unbf(in1[i]); }
  STAMP(0, 1);
  fwd_main(P, x, T, lds + 8192, lds + 8192 + 16384, XF, lane, w, b);
  STAMP(0, 4);
  STAMP_END(0, 7);
}

constexpr float GA = -0.4686686930532063f, GC = 4.91222953926424f;
DI f32x2 gelu2(f32x2 u) {
  const f32x2 z = u * (u * u + GC);
  f32x2 e; e.x = __builtin_amdgcn_exp2f(z.x); e.y = __builtin_amdgcn_exp2f(z.y);
  const f32x2 d = e + 1.0f; f32x2 r; r.x = __builtin_amdgcn_rcpf(d.x); r.y = __builtin_amdgcn_rcpf(d.y);
  return u * r;
}
template <int OFF, int AUX> DI void glds16o(const char* src, LAS char* dst) { __builtin_amdgcn_global_load_lds((const GAS void*)src, (LAS void*)dst, 16, OFF, AUX); }
#define SIG1 2
#define W2CHK 1
#define MORD 2
#define PREADR 1
#define CPRIO 0
#define ROT 0
#define AUXA SC1
#define AUXB SC1
#define PSCOPE __HIP_MEMORY_SCOPE_AGENT
#define PSLEEP 2
#define GSLEEP 2
DI void signal(unsigned* c) { __hip_atomic_fetch_add(c, 1u, __ATOMIC_RELAXED, __HIP_MEMORY_SCOPE_AGENT); }
DI unsigned peek(unsigned* c) { return __builtin_amdgcn_readfirstlane(__hip_atomic_load(c, __ATOMIC_RELAXED, PSCOPE)); }
DI void wait_cnt(unsigned* c, unsigned target) {
#pragma unroll 1
  for (int spin = 0; spin < (1 << 15); ++spin) { if (peek(c) >= target) break; __builtin_amdgcn_s_sleep(PSLEEP); }
  asm volatile("" ::: "memory");
}
DI void arrive(unsigned* c, unsigned* go, int nrep, int lane) {
  unsigned old = 0;
  if (lane == 0) old = __hip_atomic_fetch_add(c, 1u, __ATOMIC_RELAXED, __HIP_MEMORY_SCOPE_AGENT);
  if (__builtin_amdgcn_readfirstlane(old) == 15u && lane < nrep) __hip_atomic_store(go + lane * 32, 1u, __ATOMIC_RELAXED, __HIP_MEMORY_SCOPE_AGENT);
}
DI void wait_go16(unsigned* go, int lane) {
#pragma unroll 1
  for (int spin = 0; spin < (1 << 15); ++spin) {
    const unsigned v = __hip_atomic_load(go + (lane & 15), __ATOMIC_RELAXED, __HIP_MEMORY_SCOPE_AGENT);
    if (__builtin_amdgcn_ballot_w64(v == 0u) == 0) break;
    __builtin_amdgcn_s_sleep(GSLEEP);
  }
  asm volatile("" ::: "memory");
}
template <int NW, int NR, int MPR> DI void sched_step() {
#pragma unroll
  for (int i = 0; i < NR; ++i) { __builtin_amdgcn_sched_group_barrier(0x8, MPR, 0); __builtin_amdgcn_sched_group_barrier(0x100, 1, 0); }
  __builtin_amdgcn_sched_group_barrier(0x8, 4 * NW - NR * MPR, 0);
}
#define SB() __builtin_amdgcn_sched_barrier(0)

constexpr int G1_USZ = 45056, G2_H = 0, G2_RING = 57344, G2_USZ = 32768, C_TAB = 155648, A_E = 135168, F_LDS = 163840;
constexpr int SC1 = 16;
#define SIGPR 1

constexpr int K2_X = 65536, K2_Q = K2_X + 16384, K2_T = K2_Q + 16384, K2_W = K2_T + 8192, PREP_L = 32768;
struct Bufs { const u16 *x0, *wr, *wi, *w2, *b1, *b2, *W1t; u16 *W2t, *out; char* ws; unsigned* cnt; };
constexpr size_t MB = 1u << 20;
constexpr unsigned OFF_XF = 14 * MB, OFF_YF = 22 * MB, OFF_A1 = 30 * MB, OFF_P = 38 * MB, OFF_H = 54 * MB, OFF_CNT = 82 * MB;

DI void col_offsets(unsigned (&o)[3], int u1, int kt, int w, int lane) {
  const unsigned wb = (u1 * 1026 + kt * 32 + 2) & ~15u;
#pragma unroll
  for (int i = 0; i < 3; ++i) {
    const int c = (3 * (w & 3) + i) * 64 + lane, u2 = (c * 21846) >> 16;
    o[i] = min(wb + u2 * 8208 + (c - 3 * u2) * 16, 2048u * 1026u - 16u);
    PIN(o[i]);
  }
}
DI void col_prefetch(const Bufs& B, unsigned lds0, const unsigned (&o)[3], int w) {
#pragma unroll
  for (int i = 0; i < 3; ++i) glds_asm(o[i], w < 4 ? B.wr : B.wi, lds0 + K2_W + (w >> 2) * 12288 + (3 * (w & 3) + i) * 1024);
}

__global__ __launch_bounds__(512) void k_main(Bufs B) {
  extern __shared__ __attribute__((aligned(1024))) char dsm[];
  LAS char* lds = (LAS char*)dsm;
  const unsigned lds0 = (unsigned)(size_t)lds;
  const int w_ = __builtin_amdgcn_readfirstlane(threadIdx.x >> 6);
  const f32x4 zero = {0.f, 0.f, 0.f, 0.f};
  const u16* W1t = B.W1t;
  const u16* W2t = B.W2t;
  f32x2 xres[8];
#define cntA (cnt + panel * 32)
#define cntH0 (cnt + (16 + 2 * panel + split) * 32)
#define cntH1 (cnt + (48 + 2 * panel + split) * 32)
#define cntP (cnt + (80 + panel) * 32)
#define YF (B.ws + OFF_YF + LAYER * 4 * MB)
#define A1 (B.ws + OFF_A1 + LAYER * 4 * MB)
#define Pp (B.ws + OFF_P + LAYER * 8 * MB)
#define H (B.ws + OFF_H + LAYER * 14 * MB)
#pragma unroll 1
  for (int layer_ = 0; layer_ < 2; ++layer_) {
  const int KID = 1 + layer_; (void)KID;
  STAMP_DECL;
  STAMP(KID, 0);
  {
    LANE_VARS;
    const int u1 = xcd, kt = loc;
    const char* XF = B.ws + OFF_XF + LAYER * 4 * MB;
    const unsigned tile = u1 * 524288 + kt * 16384;
    unsigned xo[2];
#pragma unroll
    for (int i = 0; i < 2; ++i) { const int r = 32 * w + 16 * i + (lane >> 2); xo[i] = tile + (r ^ ((r >> 6) & 1)) * 64 + (lane & 3) * 16; PIN(xo[i]); }
    auto load_x = [&]() {
#pragma unroll
      for (int i = 0; i < 2; ++i) glds_asm_sc1(xo[i], XF, lds0 + K2_X + (2 * w + i) * 1024);
    };
    if (LAYER == 0) {
      glds_asm((8 + (w < 6 ? w : 5)) * 1024 + lane * 16, g_tab, lds0 + K2_T + (w < 6 ? w : 5) * 1024);
      load_x();
      unsigned wo[3];
      col_offsets(wo, u1, kt, w, lane);
      bar();
      col_prefetch(B, lds0, wo, w);
    }
    float tc[8]; f32x2 ts[8];
#pragma unroll
    for (int i = 0; i < 8; ++i) {
      const float a = (float)((2 * w + (i >> 2)) * (8 * ((i >> 1) & 1) + 2 * g + (i & 1))) * (1.0f / 256.0f);
      tc[i] = cosr(a); const float sn = sinr(a); ts[i] = (f32x2){sn, -sn};
      PIN(tc[i]); PIN(ts[i]);
    }
    int xa[2];
#pragma unroll
    for (int i = 0; i < 2; ++i) { xa[i] = K2_X + (64 * g + ((2 * w + i) ^ (g & 1))) * 64 + x16 * 4; PIN(xa[i]); }
    int fa = K2_W + (2 * w + 32 * g) * 48 + ((2 * u1 + 2) & 15) + 2 * x16;
    int qw = (2 * g) * 1024 + (w >> 1) * 256 + x16 * 16 + (w & 1) * 8;
    int qr = (2 * w) * 1024 + lane * 16, ow = K2_Q + (32 * g + 2 * w) * 64 + x16 * 4;
    unsigned yo = tile + qr;
    PIN(fa); PIN(qw); PIN(qr); PIN(ow); PIN(yo);
    float fr[8], fi[8];
    auto load_f = [&]() {
#pragma unroll
      for (int i = 0; i < 8; ++i) {
        const LAS char* f = lds + fa + ((i >> 2) + 16 * (8 * ((i >> 1) & 1) + (i & 1))) * 48;
        fr[i] = bflo(*(const LAS u16*)f); fi[i] = bflo(*(const LAS u16*)(f + 12288));
        PIN(fr[i]); PIN(fi[i]);
      }
    };
    if (LAYER == 0) { wait_vm<5>(); bar(); }
    u32x4 T[6];
#pragma unroll
    for (int i = 0; i < 6; ++i) { T[i] = *(const LAS u32x4*)(lds + K2_T + i * 1024 + lane * 16); PIN(T[i]); }
    if (LAYER == 0) {
      wait_vm<3>();
      bar();
      prep_load<1>(B.w2, blk * 7 + (w < 7 ? w : 6), lds0 + PREP_L + (w < 7 ? w : 6) * 4096, lane);
    } else {
      load_f();
      if (w == 7) wait_go16(B.cnt + (136 + xcd) * 32, lane);
      bar();
      load_x();
      wait_vm<0>();
      bar();
    }
    STAMP(KID, 9);
    LAS char* Q = lds + K2_Q;
    LAS char* Q2 = lds + K2_X;
    {
      u32x2 o[4];
#pragma unroll
      for (int i = 0; i < 2; ++i) {
        u32x4 d;
#pragma unroll
        for (int e = 0; e < 4; ++e) d[e] = *(const LAS unsigned*)(lds + xa[i] + e * 1024);
        const f32x4 p0 = mfma_h(T[0], d, zero), p1 = mfma_h(T[1], d, zero);
        const f32x2 v[4] = {lo2(p0), hi2(p0), lo2(p1), hi2(p1)};
#pragma unroll
        for (int e = 0; e < 4; ++e) o[e][i] = pk_f16v(v[e] * tc[4 * i + e] + swp(v[e]) * ts[4 * i + e]);
      }
#pragma unroll
      for (int e = 0; e < 4; ++e) *(LAS u32x2*)(Q + qw + (8 * (e >> 1) + (e & 1)) * 1024) = o[e];
    }
    wait_vm<4>();
    lds_barrier();
    if (LAYER == 0) load_f();
    {
      u32x2 o[4];
#pragma unroll
      for (int i = 0; i < 2; ++i) {
        const u32x4 q = *(const LAS u32x4*)(Q + qr + i * 1024);
        const f32x4 z0 = mfma_h(T[0], q, zero), z1 = mfma_h(T[1], q, zero);
        const f32x2 z[4] = {lo2(z0), hi2(z0), lo2(z1), hi2(z1)};
        u32x4 y;
#pragma unroll
        for (int e = 0; e < 4; ++e) y[e] = pk_bf16v(z[e] * fr[4 * i + e] + swp(z[e]) * (f32x2){-fi[4 * i + e], fi[4 * i + e]});
        const f32x4 p0 = mfma_b(T[2], y, zero), p1 = mfma_b(T[3], y, zero);
        const f32x2 v[4] = {lo2(p0), hi2(p0), lo2(p1), hi2(p1)};
#pragma unroll
        for (int e = 0; e < 4; ++e) o[e][i] = pk_bf16v(v[e] * tc[4 * i + e] - swp(v[e]) * ts[4 * i + e]);
      }
#pragma unroll
      for (int e = 0; e < 4; ++e) *(LAS u32x2*)(Q2 + qw + (8 * (e >> 1) + (e & 1)) * 1024) = o[e];
    }
    lds_barrier();
#pragma unroll
    for (int i = 0; i < 2; ++i) {
      const u32x4 q = *(const LAS u32x4*)(Q2 + qr + i * 1024);
      const f32x4 z0 = mfma_b(T[4], q, zero), z1 = mfma_b(T[5], q, zero);
      const f32x2 z[4] = {lo2(z0), hi2(z0), lo2(z1), hi2(z1)};
#pragma unroll
      for (int e = 0; e < 4; ++e) *(LAS unsigned*)(lds + ow + (i + 16 * (8 * (e >> 1) + (e & 1))) * 64) = pk_bf16v(z[e]);
    }
    lds_barrier();
    const rsrc_t ry = mk_rsrc(YF, 4194304);
#pragma unroll
    for (int i = 0; i < 2; ++i) bst128(ry, yo + i * 1024, *(const LAS u32x4*)(Q + qr + i * 1024));
    wait_vm<0>();
    bar();
    if (w == 0) arrive(cnt + (96 + panel) * 32, cnt + 128 * 32 + panel, 8, lane);
    STAMP(KID, 10);
  }
  {
    LANE_VARS;
    glds_asm((14 + w) * 1024 + lane * 16, g_tab, lds0 + w * 1024);
    glds_asm((22 + (w & 3)) * 1024 + lane * 16, g_tab, lds0 + (8 + (w & 3)) * 1024);
    if (LAYER == 0) {
      glds_asm((256 * w + b) * 2048 + co, B.x0, lds0 + K2_X + w * 2048);
      glds_asm((256 * w + b) * 2048 + co + 1024, B.x0, lds0 + K2_X + w * 2048 + 1024);
      if (w < 7) prep_store<1>(B.W2t, blk * 7 + w, lds + PREP_L + w * 4096, lane);
    }
    f32x2 tc[4], ts[4];
#pragma unroll
    for (int i = 0; i < 8; ++i) { const float a = (float)((8 * (x16 >> 2) + 4 * (i >> 2) + (x16 & 3)) * (4 * g + (i & 3))) * (1.0f / 1024.0f); tc[i >> 1][i & 1] = cosr(a); ts[i >> 1][i & 1] = sinr(a); }
    u32x2 M;
    {
      const int s = 256 * (x16 >> 1) + b, op = x16 & 1;
      const float a0 = (float)(((2 * g) * s) & 2047) * (1.0f / 2048.0f), a1 = (float)(((2 * g + 1) * s) & 2047) * (1.0f / 2048.0f);
      const float c0 = cosr(a0), s0 = sinr(a0), c1 = cosr(a1), s1 = sinr(a1);
      M.x = op ? pk_bf16(s0, c0) : pk_bf16(c0, -s0);
      M.y = op ? pk_bf16(s1, c1) : pk_bf16(c1, -s1);
    }
    const int kq = 4 * (x16 & 7), kh3 = 2 * w + ((x16 + 1) >> 3);
    int ea[4], eb3, am; unsigned sg[2], mk;
#pragma unroll
    for (int t = 0; t < 4; ++t) {
      const int kl = (kq + t + 1) & 31, kh = t < 3 ? 2 * w + (x16 >> 3) : kh3; const bool mir = kl > 16;
      const int k1 = mir ? 32 - kl : kl, k2 = mir ? 31 - kh : kh;
      ea[t] = (2 * g) * 2064 + (k2 >> 2) * 256 + k1 * 16 + (k2 & 3) * 4;
    }
    eb3 = ea[3] + 2064;
    if (kq == 12) { ea[3] = 8 * 2064 + (2 * g) * 64 + kh3 * 4; eb3 = ea[3] + 64; }
    sg[0] = kq >= 16 ? 0x80000000u : 0u; sg[1] = (kq >= 16 && kq < 28) ? 0x80000000u : 0u;
    const bool nyq = kh3 == 16;
    mk = nyq ? 0xffffu : 0xffffffffu;
    { const int m2 = nyq ? 0 : 32 - kh3; am = (2 * g) * 2064 + (m2 >> 2) * 256 + (m2 & 3) * 4; }
#pragma unroll
    for (int i = 0; i < 4; ++i) { PIN(tc[i]); PIN(ts[i]); PIN(ea[i]); }
    const int rr = idx * 8 + w;
    unsigned yo = (2 * g) * 524288 + (4 * w + (x16 >> 2)) * 16384 + b * 64 + (x16 & 3) * 16, wo = idx * (16 * 28672) + w * 1024 + lane * 16;
    unsigned ad = panel * 262144 + rr * 128 + (x16 >> 1) * 16384 + ((((x16 & 1) * 4 + g) ^ ((rr >> 1) & 7)) * 16);
    int er = A_E + w * 2064 + lane * 16, er16 = A_E + 8 * 2064 + w * 64 + g * 16;
    PIN(M); PIN(eb3); PIN(am); PIN(sg[0]); PIN(sg[1]); PIN(mk); PIN(yo); PIN(wo); PIN(ad); PIN(er); PIN(er16);
    if (LAYER != 0) wait_vm<0>(); else if (w < 7) wait_vm<6>(); else wait_vm<2>();
    bar();
    u32x4 Tf[12];
#pragma unroll
    for (int i = 0; i < 12; ++i) { Tf[i] = *(const LAS u32x4*)(lds + i * 1024 + lane * 16); PIN(Tf[i]); }
    if (LAYER == 0) {
      if (w < 7) wait_vm<4>(); else wait_vm<0>();
#pragma unroll
      for (int i = 0; i < 2; ++i) {
        const u32x4 r = *(const LAS u32x4*)(lds + K2_X + w * 2048 + i * 1024 + lane * 16);
#pragma unroll
        for (int e = 0; e < 4; ++e) xres[4 * i + e] = unbf(r[e]);
      }
    }
    if (w == 7) wait_go16(cnt + (128 + xcd) * 32, lane);
    bar();
    u32x4 d0, d1;
    {
      asm volatile("s_nop 4\n\tglobal_load_dwordx4 %0, %2, %4 sc1\n\tglobal_load_dwordx4 %1, %3, %4 sc1" : "=&v"(d0), "=&v"(d1) : "v"(yo), "v"(yo + 524288), "s"(YF) : "memory");
    }
    asm volatile("s_waitcnt vmcnt(0)" : "+v"(d0), "+v"(d1)::"memory");
    STAMP(KID, 11);
    LAS char* E = lds + A_E;
#pragma unroll
    for (int t = 0; t < 4; ++t) {
      const f32x4 r = mfma16_b(M, (u32x2){d0[t], d1[t]}, zero);
      unsigned v0 = pk_bf16v(lo2(r)) ^ sg[t == 3], v1 = pk_bf16v(hi2(r)) ^ sg[t == 3];
      if (t == 3) { v0 &= mk; v1 &= mk; }
      *(LAS unsigned*)(E + ea[t]) = v0;
      *(LAS unsigned*)(E + (t < 3 ? ea[t] + 2064 : eb3)) = v1;
      if (t == 3 && kq == 28) {
        *(LAS unsigned*)(E + am) = nyq ? 0u : v0 ^ 0x80000000u;
        *(LAS unsigned*)(E + am + 2064) = nyq ? 0u : v1 ^ 0x80000000u;
      }
    }
    {
#pragma unroll
      for (int i = 0; i < 3; ++i) glds_asm(wo + i * 8192, W1t, lds0 + 16384 + (w + 8 * i) * 1024);
      if (w < 4) glds_asm(wo + 3 * 8192, W1t, lds0 + 16384 + (w + 24) * 1024);
    }
    lds_barrier();
    const u32x4 A0 = *(const LAS u32x4*)(lds + er), A1f = *(const LAS u32x4*)(lds + er + 1024);
    u32x4 A16 = *(const LAS u32x4*)(lds + er16);
    if (x16 != 0) A16 = (u32x4){0u, 0u, 0u, 0u};
    f32x4 y[2][2];
    {
      u32x4 G[2];
#pragma unroll
      for (int c = 0; c < 2; ++c) {
        f32x4 gr = mfma_b(A0, Tf[c], zero); gr = mfma_b(A1f, Tf[2 + c], gr);
        f32x4 gi = mfma_b(A0, Tf[4 + c], zero); gi = mfma_b(A1f, Tf[6 + c], gi); gi = mfma_b(A16, Tf[8 + c], gi);
        G[c] = (u32x4){pk_bf16v(lo2(gr) * tc[2 * c] - lo2(gi) * ts[2 * c]), pk_bf16v(hi2(gr) * tc[2 * c + 1] - hi2(gi) * ts[2 * c + 1]),
                       pk_bf16v(lo2(gi) * tc[2 * c] + lo2(gr) * ts[2 * c]), pk_bf16v(hi2(gi) * tc[2 * c + 1] + hi2(gr) * ts[2 * c + 1])};
      }
#pragma unroll
      for (int e = 0; e < 2; ++e)
#pragma unroll
        for (int c = 0; c < 2; ++c) y[e][c] = (f32x4){xres[4 * e + 2 * c].x, xres[4 * e + 2 * c].y, xres[4 * e + 2 * c + 1].x, xres[4 * e + 2 * c + 1].y};
#pragma unroll
      for (int e = 0; e < 2; ++e) { y[e][0] = mfma_b(G[0], Tf[10 + e], y[e][0]); y[e][1] = mfma_b(G[1], Tf[10 + e], y[e][1]); }
    }
#pragma unroll
    for (int i = 0; i < 8; ++i) xres[i] = (i & 1) ? hi2(y[i >> 2][(i >> 1) & 1]) : lo2(y[i >> 2][(i >> 1) & 1]);
    float rden, nm;
    ln_stats<true>(xres, rden, nm);
    rden *= GA; nm *= GA;
    const rsrc_t ra = mk_rsrc(A1, 4194304);
#pragma unroll
    for (int e = 0; e < 2; ++e) {
      u32x4 o;
#pragma unroll
      for (int h = 0; h < 4; ++h) o[h] = pk_bf16v(xres[4 * e + h] * rden + nm);
      bst128(ra, ad + e * 8 * 16384, o);
    }
    STAMP(KID, 8);
    const u32x4 bb0 = ldg<u32x4>(B.b2, co), bb1 = ldg<u32x4>(B.b2, co + 1024);
    wait_vm<0>();
    bar();
    if (w == 0) arrive(cntA, cnt + 144 * 32 + panel, 8, lane);
#pragma unroll
    for (int e = 0; e < 4; ++e) { xres[e] += unbf(bb0[e]); xres[4 + e] += unbf(bb1[e]); PIN(xres[e]); PIN(xres[4 + e]); }
    STAMP(KID, 1);
  }
  volatile LAS unsigned* done = (volatile LAS unsigned*)(lds + A_E);
  if (w_ >= 4) {
    {
      LANE_VARS;
      const int lw = w - 4;
      const char* Ab = (const char*)A1 + (size_t)panel * 262144 + lw * 4096 + lane * 16;
      const char* Wb = (const char*)W1t + (size_t)idx * 16 * 28672 + lw * 7168 + lane * 16;
      auto issueA = [&](int u, int slot) {
        const char* a = Ab + u * 16384; LAS char* la = lds + slot * G1_USZ + lw * 4096;
        glds16o<0, SC1>(a, la); glds16o<1024, SC1>(a, la); glds16o<2048, SC1>(a, la); glds16o<3072, SC1>(a, la);
      };
      auto issueB = [&](int u, int slot) {
        const char* wp = Wb + u * 28672; LAS char* lb = lds + slot * G1_USZ + 16384 + lw * 7168;
        glds16o<0, 0>(wp, lb); glds16o<1024, 0>(wp, lb); glds16o<2048, 0>(wp, lb); glds16o<3072, 0>(wp, lb);
        glds16o<0, 0>(wp + 4096, lb + 4096); glds16o<1024, 0>(wp + 4096, lb + 4096); glds16o<2048, 0>(wp + 4096, lb + 4096);
      };
      wait_cnt(cntA, 16);
      issueA(0, 0); issueA(1, 1); issueB(1, 1);
      wait_vm<11>(); bar();
      issueA(2, 2); issueB(2, 2);
      int slot = 0;
      auto step = [&](int u) {
        wait_vm<11>(); bar();
        issueA(u + 2, slot); issueB(u + 2, slot);
        slot = slot == 2 ? 0 : slot + 1;
      };
#pragma unroll 1
      for (int u = 1; u <= 8; ++u) step(u);
      {
        unsigned go;
        asm volatile("s_nop 4\n\tglobal_load_dword %0, %1, %2 sc1" : "=&v"(go) : "v"(x16 * 4), "s"(B.cnt + (144 + xcd) * 32) : "memory");
        step(9); step(10);
        asm volatile("" : "+v"(go)::"memory");
        if (__builtin_amdgcn_ballot_w64(go == 0u) != 0) wait_go16(B.cnt + (144 + xcd) * 32, lane);
      }
#pragma unroll 1
      for (int u = 11; u <= 13; ++u) step(u);
      wait_vm<11>(); bar();
      wait_vm<0>(); bar();
      bar();
    }
    {
      LANE_VARS;
      const int lw = w - 4;
      const char* Hb = (const char*)H + ((size_t)panel * 112 + 56 * split) * 8192 + lw * 2048 + lane * 16;
      const char* Wb = (const char*)W2t + ((size_t)j2 * 112 + 56 * split) * 8192 + lw * 2048 + lane * 16;
      auto qof = [&](int s) {
        int pr, ql;
        if (s < 7) { pr = 0; ql = s; }
        else if (s < 21) { const int t = s - 7; pr = 1 + (t >> 1); ql = 3 * (t & 1); }
        else { const int t = s - 21; pr = 1 + (t * 52 >> 8); const int r5 = t - 5 * (pr - 1); ql = r5 + 1 + (r5 >= 2 ? 1 : 0); }
        return 7 * (pr == 0 ? j2 : pr - 1 + (pr > j2 ? 1 : 0)) + ql;
      };
      auto issueq = [&](int q, int s, int slot, bool withA) {
        LAS char* l = lds + G2_RING + slot * G2_USZ + (s & 1) * 8192 + lw * 2048;
        if (withA) { glds16o<0, AUXA>(Hb + q * 8192, l); glds16o<1024, AUXA>(Hb + q * 8192, l); }
        glds16o<0, AUXB>(Wb + q * 8192, l + 16384); glds16o<1024, AUXB>(Wb + q * 8192, l + 16384);
      };
      auto issue = [&](int s, int slot, bool withA) { issueq(qof(s), s, slot, withA); };
#pragma unroll
      for (int s = 0; s < 6; ++s) issue(s, s >> 1, false);
      if (LAYER == 0) { glds16o<0, 0>((const char*)g_tab + lw * 2048 + lane * 16, lds + C_TAB + lw * 2048); glds16o<1024, 0>((const char*)g_tab + lw * 2048 + lane * 16, lds + C_TAB + lw * 2048); }
      bool ok = false;
      wait_vm<0>();
      unsigned early;
      {
#pragma unroll 1
        for (int spin = 0; spin < (1 << 20); ++spin) { if (__builtin_amdgcn_readfirstlane(*done) == 0x5eedu) break; __builtin_amdgcn_s_sleep(1); }
        asm volatile("s_nop 4\n\tglobal_load_dword %0, %1, %2 sc1" : "=&v"(early) : "v"(0u), "s"(cntH0) : "memory");
      }
      bar();
      bar();
      asm volatile("s_waitcnt vmcnt(0)" : "+v"(early)::"memory");
      ok = __builtin_amdgcn_readfirstlane(early) >= 32;
      if (!ok) wait_cnt(cntH0, 32);
      issue(6, 0, false); issue(7, 0, true);
      bar();
      issue(8, 1, true); issue(9, 1, true);
      int slot = 2;
#pragma unroll 1
      for (int u = 3; u <= 25; ++u) {
        int q0 = qof(2 * u + 4), q1 = qof(2 * u + 5);
        asm volatile("" : "+s"(q0), "+s"(q1));
        wait_vm<8>(); bar();
        issueq(q0, 0, slot, true); issueq(q1, 1, slot, true);
        slot = slot == 2 ? 0 : slot + 1;
      }
      wait_vm<8>(); bar();
      wait_vm<0>(); bar();
    }
  } else {
    {
      LANE_VARS;
      const int wm = w >> 1, wn = w & 1;
      f32x4 acc[4][7];
      u32x2 bt[7];
#pragma unroll
      for (int i = 0; i < 7; ++i) bt[i] = ldg<u32x2>(B.b1, (idx * 224 + (i < 6 ? 6 * wn + i : 12 + wn) * 16 + 4 * g) * 2);
      asm volatile("" : "+v"(bt[0]), "+v"(bt[1]), "+v"(bt[2]), "+v"(bt[3]), "+v"(bt[4]), "+v"(bt[5]), "+v"(bt[6]));
#pragma unroll
      for (int i = 0; i < 7; ++i) {
        const f32x4 bv = (f32x4){bflo(bt[i].x), bfhi(bt[i].x), bflo(bt[i].y), bfhi(bt[i].y)} * GA;
#pragma unroll
        for (int mt = 0; mt < 4; ++mt) { acc[mt][i] = bv; PIN(acc[mt][i]); }
      }
      const unsigned aoff0 = lds0 + (wm * 64 + x16) * 128 + ((g ^ ((x16 >> 1) & 7)) * 16), aoff1 = aoff0 ^ 64;
      const unsigned boff = lds0 + 16384 + wn * 6144 + lane * 16, boffh = lds0 + 16384 + (12 + wn) * 1024 + lane * 16;
      u32x4 fa[2][4], fb[2][7];
      unsigned ad[4];
      auto adr = [&](unsigned so) { ad[0] = aoff0 + so; ad[1] = aoff1 + so; ad[2] = boff + so; ad[3] = boffh + so;
        PIN(ad[0]); PIN(ad[2]); PIN(ad[3]);
      };
      auto rd = [&](int buf, int ks) {
        const LAS char* pa = (const LAS char*)(size_t)ad[ks];
        const LAS char* pb = (const LAS char*)(size_t)ad[2];
        const LAS char* ph = (const LAS char*)(size_t)ad[3];
        fb[buf][0] = *(const LAS u32x4*)(pb + ks * 14336);
#pragma unroll
        for (int mt = 0; mt < 4; ++mt) fa[buf][mt] = *(const LAS u32x4*)(pa + mt * 2048);
#pragma unroll
        for (int i = 1; i < 7; ++i) fb[buf][i] = *(const LAS u32x4*)(i == 6 ? ph + ks * 14336 : pb + ks * 14336 + i * 1024);
      };
      auto mm = [&](int buf) {
#pragma unroll
        for (int i = 0; i < 7; ++i)
#pragma unroll
          for (int mm_ = 0; mm_ < 4; ++mm_) { const int mt = (i & 1) ? 3 - mm_ : mm_; acc[mt][i] = mfma_b(fb[buf][i], fa[buf][mt], acc[mt][i]); }
      };
      adr(0);
      lds_barrier();
      STAMP(KID, 2);
      CLK(0);
      rd(0, 0);
      unsigned so = 0;
#pragma unroll 1
      for (int u = 0; u < 15; ++u) {
        SB(); rd(1, 1); mm(0); sched_step<7, 11, 2>(); SB();
        so = so == 2 * G1_USZ ? 0u : so + G1_USZ;
        adr(so);
        lds_barrier();
        SB(); rd(0, 0); mm(1); sched_step<7, 11, 2>();
      }
      SB(); rd(1, 1); mm(0); sched_step<7, 11, 2>(); SB();
      if (threadIdx.x == 0) *done = 0u;
      lds_barrier();
      SB(); mm(1); SB();
      STAMP(KID, 3);
      CLK(1);
      const rsrc_t rh = mk_rsrc(H, 14680064);
      const int lane2 = opaque(threadIdx.x) & 63;
      const unsigned hb = (panel * 112 + idx * 7) * 8192 + (wm * 4) * 1024 + lane2 * 16;
      LAS char* hl = lds + G2_H + (wm * 4) * 1024 + lane2 * 16;
#pragma unroll
      for (int pr = 0; pr < 3; ++pr) {
        const int q = 3 * wn + pr;
#pragma unroll
        for (int mt = 0; mt < 4; ++mt) {
          const f32x4 x0 = acc[mt][2 * pr], x1 = acc[mt][2 * pr + 1];
          const u32x4 o = {pk_bf16v(gelu2(lo2(x0))), pk_bf16v(gelu2(hi2(x0))), pk_bf16v(gelu2(lo2(x1))), pk_bf16v(gelu2(hi2(x1)))};
          bst128(rh, hb + q * 8192 + mt * 1024, o);
          *(LAS u32x4*)(hl + q * 8192 + mt * 1024) = o;
        }
        SB();
        if (pr == SIGPR) {
          wait_vm<4 * SIGPR>();
          if (lane == 0) signal(cntH0);
        }
      }
      if (lane == 0) *done = 0x5eedu;
#pragma unroll
      for (int mt = 0; mt < 4; ++mt) {
        const f32x4 x0 = acc[mt][6];
        const u32x2 o = {pk_bf16v(gelu2(lo2(x0))), pk_bf16v(gelu2(hi2(x0)))};
        bst64(rh, hb + 6 * 8192 + mt * 1024 + wn * 8, o);
        *(LAS u32x2*)(hl + 6 * 8192 + mt * 1024 + wn * 8) = o;
      }
    }
    {
      LANE_VARS;
      const int wm = w >> 1, wn = w & 1;
      f32x4 acc[4][4];
#pragma unroll
      for (int i = 0; i < 4; ++i)
#pragma unroll
        for (int mt = 0; mt < 4; ++mt) { acc[mt][i] = zero; PIN(acc[mt][i]); }
      const unsigned aoff = lds0 + wm * 4096 + lane * 16, boff = lds0 + G2_RING + 16384 + wn * 4096 + lane * 16;
      u32x4 fa[2][4], fb[2][4];
      auto rd = [&](int buf, unsigned ao, unsigned bo) {
        const LAS char* pa = (const LAS char*)(size_t)(aoff + ao);
        const LAS char* pb = (const LAS char*)(size_t)(boff + bo);
        fb[buf][0] = *(const LAS u32x4*)pb;
#pragma unroll
        for (int mt = 0; mt < 4; ++mt) fa[buf][mt] = *(const LAS u32x4*)(pa + mt * 1024);
#pragma unroll
        for (int i = 1; i < 4; ++i) fb[buf][i] = *(const LAS u32x4*)(pb + i * 1024);
      };
      auto mm = [&](int buf) {
#pragma unroll
        for (int i = 0; i < 4; ++i)
#pragma unroll
          for (int mm_ = 0; mm_ < 4; ++mm_) { const int mt = (i & 1) ? 3 - mm_ : mm_; acc[mt][i] = mfma_b(fb[buf][i], fa[buf][mt], acc[mt][i]); }
      };
      unsigned so = 0;
      auto unit = [&](int u, bool early) {
        SB(); rd(1, early && u < 3 ? G2_H + (2 * u + 1) * 8192 : G2_RING + so + 8192, so + 8192); mm(0); sched_step<4, 8, 1>(); SB();
        so = so == 2 * G2_USZ ? 0u : so + G2_USZ;
        unsigned na = aoff + (early && u < 3 ? G2_H + (2 * u + 2) * 8192 : G2_RING + so), nb = boff + so;
        PIN(na); PIN(nb);
        lds_barrier();
        SB(); rd(0, na - aoff, nb - boff); mm(1); sched_step<4, 8, 1>(); SB();
      };
      lds_barrier();
      STAMP(KID, 4);
      CLK(2);
      rd(0, G2_H, 0);
      unit(0, true); unit(1, true);
      unit(2, true);
      wait_vm<0>();
      if (lane == 0) signal(cntH1);
      unit(3, false); unit(4, false);
      unsigned late = __hip_atomic_load(cntH1, __ATOMIC_RELAXED, __HIP_MEMORY_SCOPE_AGENT);
      unit(5, false); unit(6, false);
      if (w == 0 && __builtin_amdgcn_readfirstlane(late) < 32) wait_cnt(cntH1, 32);
#pragma unroll 1
      for (int u = 7; u < 27; ++u) unit(u, false);
      SB(); rd(1, G2_RING + so + 8192, so + 8192); mm(0); sched_step<4, 8, 1>(); SB();
      mm(1);
      STAMP(KID, 5); CLK(3);
      const rsrc_t ro = mk_rsrc(Pp, 8388608);
      const unsigned ob = split * 4194304 + (panel * 128 + wm * 64 + x16) * 2048 + (j2 * 128 + wn * 64 + g * 8) * 2;
#pragma unroll
      for (int pr = 0; pr < 2; ++pr)
#pragma unroll
        for (int mt = 0; mt < 4; ++mt) {
          const f32x4 x0 = acc[mt][2 * pr], x1 = acc[mt][2 * pr + 1];
          bst128(ro, ob + mt * 32768 + pr * 64, (u32x4){pk_bf16v(lo2(x0)), pk_bf16v(hi2(x0)), pk_bf16v(lo2(x1)), pk_bf16v(hi2(x1))});
        }
    }
  }
  LANE_VARS;
  FwdPre P;
  fwd_pre(P, lane, w, b);
  unsigned wo[3], po = (8 * b + w) * 2048 + co;
  col_offsets(wo, xcd, loc, w, lane);
  PIN(po);
  u32x4 T[8];
#pragma unroll
  for (int i = 0; i < 8; ++i) { T[i] = *(const LAS u32x4*)(lds + C_TAB + i * 1024 + lane * 16); PIN(T[i]); }
  STAMPT(KID, 13, 64);
  wait_vm<0>();
  lds_barrier();
  STAMPT(KID, 14, 64);
  if (w == 0) { if (lane == 0) signal(cntP); wait_cnt(cntP, 16); }
  bar();
  STAMP(KID, 6);
  {
    const rsrc_t rp = mk_rsrc(Pp, 8388608);
    u32x4 p[2][2];
#pragma unroll
    for (int i = 0; i < 2; ++i) { p[0][i] = __builtin_amdgcn_raw_buffer_load_b128(rp, po + 1024 * i, 0, SC1); p[1][i] = __builtin_amdgcn_raw_buffer_load_b128(rp, po + 4194304 + 1024 * i, 0, SC1); }
#pragma unroll
    for (int i = 0; i < 8; ++i) { xres[i] += (unbf(p[0][i >> 2][i & 3]) + unbf(p[1][i >> 2][i & 3])) * (1.0f / GA); PIN(xres[i]); }
    STAMP(KID, 12);
    if (LAYER == 0) {
      glds_asm((8 + (w < 6 ? w : 5)) * 1024 + lane * 16, g_tab, lds0 + K2_T + (w < 6 ? w : 5) * 1024);
      col_prefetch(B, lds0, wo, w);
      fwd_main(P, xres, T, lds, lds + 16384, (unsigned*)(B.ws + OFF_XF + 4 * MB), lane, w, b);
      STAMP(KID, 15);
      wait_vm<0>();
      bar();
      if (w == 0) arrive(cnt + (112 + panel) * 32, cnt + 136 * 32 + panel, 8, lane);
    } else {
      const rsrc_t rx = mk_rsrc(B.out, 4194304);
#pragma unroll
      for (int i = 0; i < 2; ++i) bst128(rx, (256 * w + b) * 2048 + co + 1024 * i, (u32x4){pk_bf16v(xres[4 * i]), pk_bf16v(xres[4 * i + 1]), pk_bf16v(xres[4 * i + 2]), pk_bf16v(xres[4 * i + 3])});
    }
  }
  STAMP_END(KID, 7);
  }
}

extern "C" void kernel_launch(void* const* d_in, const int* in_sizes, int n_in, void* d_out, int out_size, void* d_ws, size_t ws_size, hipStream_t stream) {
  char* ws = (char*)d_ws;
  Bufs B;
  B.x0 = (const u16*)d_in[0]; B.wr = (const u16*)d_in[1]; B.wi = (const u16*)d_in[2];
  B.w2 = (const u16*)d_in[6]; B.b1 = (const u16*)d_in[5]; B.b2 = (const u16*)d_in[7];
  B.W1t = (const u16*)ws; B.W2t = (u16*)(ws + 7 * MB); B.out = (u16*)d_out; B.ws = ws; B.cnt = (unsigned*)(ws + OFF_CNT);
  (void)hipFuncSetAttribute((const void*)k_main, hipFuncAttributeMaxDynamicSharedMemorySize, F_LDS);
  k_fwd<<<8 + 256 + NPREP, 512, 0, stream>>>(B.x0, (unsigned*)(ws + OFF_XF), (const u16*)d_in[4], (u16*)ws, B.cnt);
  k_main<<<256, 512, F_LDS, stream>>>(B);
}
```
